```python
import torch

def _softmax(x, axis=-1):
    return torch.softmax(x, dim=axis)
def _gelu(x):
    return torch.nn.functional.gelu(x, approximate="tanh")
def _silu(x):
    return torch.nn.functional.silu(x)
def _sigmoid(x):
    return torch.sigmoid(x)
def _layernorm(x, axis=-1, eps=1e-5):
    m = x.mean(dim=axis, keepdim=True)
    v = x.var(dim=axis, keepdim=True, unbiased=False)
    return (x - m) / torch.sqrt(v + eps)
def _scatter_add(tgt, idx, src, axis=0):
    return tgt.clone().index_add_(axis, idx, src)
def _segment_sum(src, seg, n):
    out = torch.zeros((n,) + src.shape[1:], dtype=src.dtype)
    return out.index_add_(0, seg, src)
def _pad(x, pw):
    flat = tuple(v for pair in reversed(pw) for v in pair)
    return torch.nn.functional.pad(x, flat)

kernel_name = 'gated_mix__mha_block__grouped_kv_mqa__prompt'

def setup_inputs():

    _w = (lambda a: (lambda t: t.to(torch.bfloat16) if t.is_floating_point() else t)(torch.as_tensor(a)))
    def _A_setup():
        B, S, D, H = 1, 2048, 640, 32
        return {
            "x": _w((lambda *s: torch.randn(*s, dtype=torch.bfloat16) * 0.1)(B, S, D)),
            "wqkv": _w((lambda *s: torch.randn(*s, dtype=torch.bfloat16) * 0.4)(D, 3 * D)),
            "wo": _w((lambda *s: torch.randn(*s, dtype=torch.bfloat16) * 0.2)(D, D)),
        }
    _A_in = _A_setup()

    def _B_setup():
        B, S, D, H = 1, 2048, 640, 32
        Dh = D // H
        return {
            "x": _w((lambda *s: torch.randn(*s, dtype=torch.bfloat16) * 0.1)(B, S, D)),
            "wq": _w((lambda *s: torch.randn(*s, dtype=torch.bfloat16) * 0.4)(D, H * Dh)),
            "wk": _w((lambda *s: torch.randn(*s, dtype=torch.bfloat16) * 0.4)(D, Dh)),
            "wv": _w((lambda *s: torch.randn(*s, dtype=torch.bfloat16) * 0.4)(D, Dh)),
            "wo": _w((lambda *s: torch.randn(*s, dtype=torch.bfloat16) * 0.4)(H * Dh, D)),
        }
    _B_in = _B_setup()

    return {
        "A_x": _A_in["x"],
        "A_wqkv": _A_in["wqkv"],
        "A_wo": _A_in["wo"],
        "B_x": _B_in["x"],
        "B_wq": _B_in["wq"],
        "B_wk": _B_in["wk"],
        "B_wv": _B_in["wv"],
        "B_wo": _B_in["wo"],
        "gate_w": _w((lambda *s: torch.randn(*s, dtype=torch.bfloat16) * 0.1)(640) * 16.0),
    }

def reference(A_x, A_wqkv, A_wo, B_x, B_wq, B_wk, B_wv, B_wo, gate_w):
    def _A_ref(x, wqkv, wo):
        B, S, D = x.shape
        H = 32
        Dh = D // H
        qkv = (lambda x, *d: x.permute(*d))((x @ wqkv).reshape(B, S, 3, H, Dh), 2, 0, 3, 1, 4)
        q, k, v = qkv[0], qkv[1], qkv[2]
        scores = torch.einsum("bhqd,bhkd->bhqk", q, k) * (Dh ** -0.5)
        p = _softmax(scores, axis=-1)
        o = torch.einsum("bhqk,bhkd->bhqd", p, v)
        return (lambda x, *d: x.permute(*d))(o, 0, 2, 1, 3).reshape(B, S, D) @ wo

    def _B_ref(x, wq, wk, wv, wo):
        B, S, D = x.shape
        H = 32
        Dh = D // H
        q = (lambda x, *d: x.permute(*d))((x @ wq).reshape(B, S, H, Dh), 0, 2, 1, 3)
        k = x @ wk
        v = x @ wv
        scores = torch.einsum("bhqd,bkd->bhqk", q, k) * (Dh ** -0.5)
        p = _softmax(scores, axis=-1)
        o = torch.einsum("bhqk,bkd->bhqd", p, v)
        return (lambda x, *d: x.permute(*d))(o, 0, 2, 1, 3).reshape(B, S, H * Dh) @ wo

    x = A_x
    g = _sigmoid((x * gate_w).sum(-1, keepdims=True))
    a = _A_ref(x, A_wqkv, A_wo)
    b = _B_ref(x, B_wq, B_wk, B_wv, B_wo)
    return g * a + (1 - g) * b
```

```cpp
#include <hip/hip_runtime.h>
#include <hip/hip_bf16.h>
#include <stdint.h>

typedef __attribute__((ext_vector_type(8))) short bf16x8;
typedef __attribute__((ext_vector_type(4))) short s16x4;
typedef __attribute__((ext_vector_type(16))) float f32x16;
typedef __attribute__((ext_vector_type(4))) float f32x4;
typedef __attribute__((ext_vector_type(4))) unsigned u32x4;
typedef __attribute__((ext_vector_type(2))) unsigned u32x2;
typedef __attribute__((address_space(3))) char* lds_ptr;
typedef __attribute__((address_space(3))) const char* lds_cptr;
#define DI __device__ __forceinline__
#define MFMA32(a, b, c) __builtin_amdgcn_mfma_f32_32x32x16_bf16((a), (b), (c), 0, 0, 0)
#define MFMA16(a, b, c) __builtin_amdgcn_mfma_f32_16x16x32_bf16((a), (b), (c), 0, 0, 0)

constexpr int SEQ = 2048, DM = 640, NH = 32, DH = 20;
constexpr int QT_BYTES = 1280;
constexpr int KVT_BYTES = 2560;
constexpr size_t QIMG_OFF = 0;
constexpr size_t QIMG_BYTES = (size_t)64 * 64 * QT_BYTES;
constexpr size_t KVIMG_OFF = QIMG_OFF + QIMG_BYTES;
constexpr size_t KVIMG_BYTES = (size_t)33 * 64 * KVT_BYTES;
constexpr size_t OIMG_OFF = KVIMG_OFF + KVIMG_BYTES;
constexpr size_t OIMG_BYTES = (size_t)128 * 2 * 20 * 1024;
constexpr size_t WOT_OFF = OIMG_OFF + OIMG_BYTES;
constexpr size_t WOT_BYTES = (size_t)40 * 2 * 20 * 1024;
constexpr size_t G_OFF = WOT_OFF + WOT_BYTES;
constexpr float QSCALE = 0.22360679774997896f * 1.4426950408889634f;

DI unsigned cvtpk(float lo, float hi) {
    typedef __attribute__((ext_vector_type(2))) __bf16 bf2;
    bf2 v; v[0] = (__bf16)lo; v[1] = (__bf16)hi;
    return __builtin_bit_cast(unsigned, v);
}
DI float rbf(float x) { return (float)(__bf16)x; }
DI float bf2f(unsigned short u) { return __uint_as_float((unsigned)u << 16); }

DI void glds16(const void* g, unsigned lds_base) {
    unsigned sv;
    asm volatile("s_mov_b32 %0, m0\n\ts_mov_b32 m0, %2\n\ts_nop 0\n\tglobal_load_lds_dwordx4 %1, off\n\ts_mov_b32 m0, %0"
                 : "=&s"(sv) : "v"(g), "s"(lds_base) : "memory");
}
DI void glds4(const void* g, unsigned lds_base) {
    unsigned sv;
    asm volatile("s_mov_b32 %0, m0\n\ts_mov_b32 m0, %2\n\ts_nop 0\n\tglobal_load_lds_dword %1, off\n\ts_mov_b32 m0, %0"
                 : "=&s"(sv) : "v"(g), "s"(lds_base) : "memory");
}
#define WAIT_BAR(N) asm volatile("s_waitcnt vmcnt(" #N ") lgkmcnt(0)\n\ts_barrier" ::: "memory")
typedef short v4i16_t __attribute__((ext_vector_type(4)));
DI s16x4 trread(lds_cptr p) {
    return __builtin_bit_cast(s16x4, __builtin_amdgcn_ds_read_tr16_b64_v4i16((__attribute__((address_space(3))) v4i16_t*)p));
}
DI bf16x8 ldsb128(lds_cptr p) { return *(const __attribute__((address_space(3))) bf16x8*)p; }

constexpr int P_XS = 16384, P_WS = 20480, P_STAGE = P_XS + P_WS;
constexpr int P_LDS = 2 * P_STAGE;

template <int MODE>
DI void proj_tile(const unsigned short* __restrict__ x, const unsigned short* __restrict__ w, const unsigned short* __restrict__ w2,
                  int ldw, int rb, char* lds, f32x16 (&acc)[5]) {
    const int tid = threadIdx.x, lane = tid & 63;
    const int wid = __builtin_amdgcn_readfirstlane(tid >> 6);
    const unsigned lds0 = (unsigned)(uintptr_t)lds;
    constexpr int NT = (MODE == 2) ? 2 : 5;
    const char* xsrc[4];
#pragma unroll
    for (int i = 0; i < 4; ++i) {
        int row = (wid * 4 + i) * 8 + (lane >> 3);
        int c = (lane & 7) ^ ((row >> 1) & 7);
        xsrc[i] = (const char*)x + (size_t)(rb * 128 + row) * (DM * 2) + c * 16;
    }
    const char* wsrc[5];
#pragma unroll
    for (int i = 0; i < 5; ++i) {
        if (MODE == 2) {
            int b = (wid * 5 + i) * 256 + lane * 4;
            int row = b / 80, wi = b % 80;
            wsrc[i] = (wi < 40) ? (const char*)w + row * 40 + wi : (const char*)w2 + row * 40 + (wi - 40);
        } else {
            int b = (wid * 5 + i) * 1024 + lane * 16;
            int row = b / 320, cb = b % 320;
            wsrc[i] = (const char*)w + (size_t)row * ldw * 2 + cb;
        }
    }
    const size_t wstep = (MODE == 2) ? (size_t)64 * 40 : (size_t)64 * ldw * 2;
    auto stage = [&](int kt, int buf) {
        unsigned base = lds0 + buf * P_STAGE;
#pragma unroll
        for (int i = 0; i < 4; ++i) glds16(xsrc[i] + kt * 128, base + (wid * 4 + i) * 1024);
#pragma unroll
        for (int i = 0; i < 5; ++i) {
            if (MODE == 2) glds4(wsrc[i] + kt * wstep, base + P_XS + (wid * 5 + i) * 256);
            else glds16(wsrc[i] + kt * wstep, base + P_XS + (wid * 5 + i) * 1024);
        }
    };
    const int r32 = lane & 31, hh = lane >> 5;
    const int xrow = wid * 32 + r32;
    const int xsw = (xrow >> 1) & 7;
    constexpr int WROW = (MODE == 2) ? 80 : 320;
    const int g16 = lane >> 4, i16 = lane & 15;
    const int wbase = (8 * (g16 >> 1) + (i16 >> 2)) * WROW + (16 * (g16 & 1) + 4 * (i16 & 3)) * 2;
#pragma unroll
    for (int t = 0; t < 5; ++t) acc[t] = f32x16{};
    stage(0, 0);
    stage(1, 1);
    for (int kt = 0; kt < 10; ++kt) {
        if (kt < 9) { WAIT_BAR(9); } else { WAIT_BAR(0); }
        lds_cptr xs = (lds_cptr)lds + (kt & 1) * P_STAGE;
        lds_cptr ws = xs + P_XS;
#pragma unroll
        for (int s = 0; s < 4; ++s) {
            bf16x8 xf = ldsb128(xs + xrow * 128 + (((2 * s + hh) ^ xsw) * 16));
#pragma unroll
            for (int t = 0; t < NT; ++t) {
                const int tcol = (MODE == 2) ? t * 40 : t * 64;
                s16x4 lo = trread(ws + wbase + s * 16 * WROW + tcol);
                s16x4 hi = trread(ws + wbase + (s * 16 + 4) * WROW + tcol);
                bf16x8 wf = __builtin_shufflevector(lo, hi, 0, 1, 2, 3, 4, 5, 6, 7);
                if (MODE == 0 || (MODE == 2 && t == 0)) acc[t] = MFMA32(wf, xf, acc[t]);
                else acc[t] = MFMA32(xf, wf, acc[t]);
            }
        }
        if (kt + 2 < 10) {
            asm volatile("s_waitcnt lgkmcnt(0)\n\ts_barrier" ::: "memory");
            stage(kt + 2, kt & 1);
        }
    }
}

DI int swap23(int r) { return (r & ~12) | ((r & 4) << 1) | ((r & 8) >> 1); }

DI int qk_off(int c, int unit_stride, int pos) {
    int u = c / 20, j = (c % 20) >> 2;
    return u * unit_stride + (j < 4 ? (j >> 1) * 512 + (j & 1) * 8 + pos * 16 : 1024 + pos * 8);
}

__global__ __launch_bounds__(256, 2) void proj_kernel(
    const unsigned short* __restrict__ x, const unsigned short* __restrict__ wqkv, const unsigned short* __restrict__ wqb,
    const unsigned short* __restrict__ wkb, const unsigned short* __restrict__ wvb, const unsigned short* __restrict__ woa,
    const unsigned short* __restrict__ wob, const unsigned short* __restrict__ gw, char* __restrict__ ws) {
    extern __shared__ __attribute__((aligned(16))) char lds[];
    const int tid = threadIdx.x, lane = tid & 63;
    const int wid = __builtin_amdgcn_readfirstlane(tid >> 6);
    const int r32 = lane & 31, hh = lane >> 5;
    const int b = blockIdx.x;
    f32x16 acc[5];
    if (b < 256) {
        const int xcd = b & 7, j = b >> 3;
        const int rb = (xcd & 1) * 8 + (j & 7), cb = (xcd >> 1) * 4 + (j >> 3);
        const int tb = rb * 4 + wid;
        const unsigned short* w = (cb < 12) ? wqkv + cb * 160 : wqb + (cb - 12) * 160;
        const int ldw = (cb < 12) ? 1920 : 640;
        if (cb >= 8 && cb < 12) {
            proj_tile<1>(x, w, nullptr, ldw, rb, lds, acc);
            char* base = ws + KVIMG_OFF + (size_t)((cb - 8) * 8) * (64 * KVT_BYTES) + tb * KVT_BYTES + 1280 + 8 * hh;
#pragma unroll
            for (int t = 0; t < 5; ++t) {
                int c = 32 * t + r32, u = c / 20, d = c % 20;
                char* p = base + u * (64 * KVT_BYTES) + d * 16;
#pragma unroll
                for (int g = 0; g < 4; ++g) {
                    u32x2 v = {cvtpk(acc[t][4 * g], acc[t][4 * g + 1]), cvtpk(acc[t][4 * g + 2], acc[t][4 * g + 3])};
                    *(u32x2*)(p + g * 320) = v;
                }
            }
        } else {
            proj_tile<0>(x, w, nullptr, ldw, rb, lds, acc);
            const bool isk = (cb >= 4 && cb < 8);
            char* base; int ustride, pos; float sc;
            if (isk) { base = ws + KVIMG_OFF + (size_t)((cb - 4) * 8) * (64 * KVT_BYTES) + tb * KVT_BYTES; ustride = 64 * KVT_BYTES; pos = swap23(r32); sc = 1.f; }
            else { int hb0 = (cb < 4) ? cb * 8 : 32 + (cb - 12) * 8; base = ws + QIMG_OFF + (size_t)hb0 * (64 * QT_BYTES) + tb * QT_BYTES; ustride = 64 * QT_BYTES; pos = r32; sc = QSCALE; }
#pragma unroll
            for (int t = 0; t < 5; ++t)
#pragma unroll
                for (int g = 0; g < 4; ++g) {
                    int off = hh ? qk_off(32 * t + 8 * g + 4, ustride, pos) : qk_off(32 * t + 8 * g, ustride, pos);
                    u32x2 v = {cvtpk(acc[t][4 * g] * sc, acc[t][4 * g + 1] * sc), cvtpk(acc[t][4 * g + 2] * sc, acc[t][4 * g + 3] * sc)};
                    *(u32x2*)(base + off) = v;
                }
        }
    } else if (b < 272) {
        const int rb = b - 256, tb = rb * 4 + wid;
        proj_tile<2>(x, wkb, wvb, 20, rb, lds, acc);
        char* base = ws + KVIMG_OFF + (size_t)32 * (64 * KVT_BYTES) + tb * KVT_BYTES;
        const int pos = swap23(r32);
#pragma unroll
        for (int g = 0; g < 3; ++g) {
            int c = 8 * g + 4 * hh;
            if (c < 20) {
                int off = hh ? qk_off(8 * g + 4, 0, pos) : qk_off(8 * g, 0, pos);
                u32x2 v = {cvtpk(acc[0][4 * g], acc[0][4 * g + 1]), cvtpk(acc[0][4 * g + 2], acc[0][4 * g + 3])};
                *(u32x2*)(base + off) = v;
            }
        }
        if (r32 < 20) {
            char* p = base + 1280 + 8 * hh + r32 * 16;
#pragma unroll
            for (int g = 0; g < 4; ++g) {
                u32x2 v = {cvtpk(acc[1][4 * g], acc[1][4 * g + 1]), cvtpk(acc[1][4 * g + 2], acc[1][4 * g + 3])};
                *(u32x2*)(p + g * 320) = v;
            }
        }
    } else {
        const int a = b - 272;
        {
            const int sub = lane & 15;
#pragma unroll
            for (int ps = 0; ps < 2; ++ps) {
                const int tok = a * 32 + wid * 8 + ps * 4 + (lane >> 4);
                float z = 0.f;
#pragma unroll
                for (int i = 0; i < 5; ++i) {
                    const int k = (i * 16 + sub) * 8;
                    bf16x8 xv = *(const bf16x8*)(x + (size_t)tok * DM + k);
                    bf16x8 gv = *(const bf16x8*)(gw + k);
#pragma unroll
                    for (int e = 0; e < 8; ++e) z += rbf(bf2f((unsigned short)xv[e]) * bf2f((unsigned short)gv[e]));
                }
#pragma unroll
                for (int o = 8; o >= 1; o >>= 1) z += __shfl_xor(z, o);
                z = rbf(z);
                float g = rbf(1.f / (1.f + __expf(-z)));
                if (sub == 0) ((float*)(ws + G_OFF))[tok] = g;
            }
        }
        for (int i = wid; i < 25; i += 4) {
            const int f = a * 25 + i;
            const int ks = f % 20, br = (f / 20) & 1, cb16 = f / 40;
            const unsigned short* wo = br ? wob : woa;
            const int col = cb16 * 16 + (lane & 15), kq = lane >> 4;
            unsigned short e[8];
#pragma unroll
            for (int jj = 0; jj < 8; ++jj) {
                int row;
                if (ks < 16) { int head = 2 * ks + (kq >> 1), h = kq & 1; row = head * 20 + ((jj < 4) ? 4 * h + jj : 8 + 4 * h + (jj - 4)); }
                else { int head = 8 * (ks - 16) + 2 * kq + (jj >> 2); row = head * 20 + 16 + (jj & 3); }
                e[jj] = wo[(size_t)row * DM + col];
            }
            u32x4 v = {(unsigned)e[0] | ((unsigned)e[1] << 16), (unsigned)e[2] | ((unsigned)e[3] << 16),
                       (unsigned)e[4] | ((unsigned)e[5] << 16), (unsigned)e[6] | ((unsigned)e[7] << 16)};
            *(u32x4*)(ws + WOT_OFF + (size_t)f * 1024 + lane * 16) = v;
        }
    }
}

constexpr int A_CHUNK = 8 * KVT_BYTES;
constexpr int A_LDS = 3 * A_CHUNK;

DI float max16(const f32x16& s) {
    float a = fmaxf(fmaxf(s[0], s[1]), s[2]);
#pragma unroll
    for (int r = 3; r < 15; r += 2) a = fmaxf(fmaxf(a, s[r]), s[r + 1]);
    return fmaxf(a, s[15]);
}
DI float xhalf_max(float m) {
    auto rr = __builtin_amdgcn_permlane32_swap(__float_as_uint(m), __float_as_uint(m), false, false);
    return fmaxf(__uint_as_float(rr[0]), __uint_as_float(rr[1]));
}

__global__ __launch_bounds__(256, 2) void attn_kernel(char* __restrict__ ws) {
    extern __shared__ __attribute__((aligned(16))) char lds[];
    const int tid = threadIdx.x, lane = tid & 63;
    const int wid = __builtin_amdgcn_readfirstlane(tid >> 6);
    const int r32 = lane & 31, hh = lane >> 5;
    const int wg = blockIdx.x;
    const int br = wg >> 8, xcd = wg & 7, j = (wg >> 3) & 31;
    const int head = (j >> 3) * 8 + xcd, qb = j & 7;
    const int hb = br * 32 + head, kvu = br ? 32 : head;
    const unsigned lds0 = (unsigned)(uintptr_t)lds;
    const char* kvsrc = ws + KVIMG_OFF + (size_t)kvu * (64 * KVT_BYTES) + wid * 5120 + lane * 16;
    auto stage = [&](int c, int slot) {
#pragma unroll
        for (int i = 0; i < 5; ++i) glds16(kvsrc + c * A_CHUNK + i * 1024, lds0 + slot * A_CHUNK + wid * 5120 + i * 1024);
    };
    stage(0, 0);
    stage(1, 1);
    const int tb0 = qb * 8 + wid * 2;
    bf16x8 q0[2], q1[2];
#pragma unroll
    for (int i = 0; i < 2; ++i) {
        const char* qp = ws + QIMG_OFF + ((size_t)hb * 64 + tb0 + i) * QT_BYTES;
        q0[i] = *(const bf16x8*)(qp + lane * 16);
        u32x2 t = *(const u32x2*)(qp + 1024 + r32 * 8);
        u32x4 u = {hh ? 0u : t[0], hh ? 0u : t[1], 0u, 0u};
        q1[i] = __builtin_bit_cast(bf16x8, u);
    }
    const int koff = lane * 16, k1off = 1024 + r32 * 8;
    const int voff = 1280 + hh * 320 + (r32 < 20 ? r32 : r32 - 16) * 16;
    const bool isone = (r32 >= 20);
    const u32x4 ones = {0x3f803f80u, 0x3f803f80u, 0x3f803f80u, 0x3f803f80u};
    auto loadk = [&](lds_cptr t, bf16x8& k0, bf16x8& k1) {
        k0 = ldsb128(t + koff);
        u32x2 a = *(const __attribute__((address_space(3))) u32x2*)(t + k1off);
        u32x4 u = {a[0], a[1], 0u, 0u};
        k1 = __builtin_bit_cast(bf16x8, u);
    };
    f32x16 o[2];
    float m[2];
    for (int pass = 0; pass < 2; ++pass) {
        if (pass == 1) {
            m[0] = -INFINITY; m[1] = -INFINITY;
            for (int c = 0; c < 8; ++c) {
                stage(c, 0);
                WAIT_BAR(0);
                for (int t = 0; t < 8; ++t) {
                    bf16x8 k0, k1;
                    loadk((lds_cptr)lds + t * KVT_BYTES, k0, k1);
#pragma unroll
                    for (int i = 0; i < 2; ++i) {
                        f32x16 s = MFMA32(k0, q0[i], f32x16{});
                        s = MFMA32(k1, q1[i], s);
                        m[i] = fmaxf(m[i], max16(s));
                    }
                }
                WAIT_BAR(0);
            }
            m[0] = xhalf_max(m[0]); m[1] = xhalf_max(m[1]);
            stage(0, 0);
            stage(1, 1);
        }
        o[0] = f32x16{}; o[1] = f32x16{};
        f32x16 negm[2];
        for (int c = 0; c < 8; ++c) {
            if (c < 7) { WAIT_BAR(5); } else { WAIT_BAR(0); }
            if (c + 2 < 8) stage(c + 2, (c + 2) % 3);
            lds_cptr cb = (lds_cptr)lds + (c % 3) * A_CHUNK;
            if (c == 0) {
                if (pass == 0) {
                    bf16x8 k0, k1;
                    loadk(cb, k0, k1);
#pragma unroll
                    for (int i = 0; i < 2; ++i) {
                        f32x16 s = MFMA32(k0, q0[i], f32x16{});
                        s = MFMA32(k1, q1[i], s);
                        m[i] = xhalf_max(max16(s));
                    }
                }
#pragma unroll
                for (int i = 0; i < 2; ++i)
#pragma unroll
                    for (int r = 0; r < 16; ++r) negm[i][r] = -m[i];
            }
#pragma unroll 2
            for (int t = 0; t < 8; ++t) {
                lds_cptr tp = cb + t * KVT_BYTES;
                bf16x8 k0, k1;
                loadk(tp, k0, k1);
                u32x4 v0 = *(const __attribute__((address_space(3))) u32x4*)(tp + voff);
                u32x4 v1 = *(const __attribute__((address_space(3))) u32x4*)(tp + voff + 640);
                if (isone) { v0 = ones; v1 = ones; }
#pragma unroll
                for (int i = 0; i < 2; ++i) {
                    f32x16 s = MFMA32(k0, q0[i], negm[i]);
                    s = MFMA32(k1, q1[i], s);
#pragma unroll
                    for (int r = 0; r < 16; ++r) s[r] = __builtin_amdgcn_exp2f(s[r]);
                    u32x4 p0 = {cvtpk(s[0], s[1]), cvtpk(s[2], s[3]), cvtpk(s[4], s[5]), cvtpk(s[6], s[7])};
                    u32x4 p1 = {cvtpk(s[8], s[9]), cvtpk(s[10], s[11]), cvtpk(s[12], s[13]), cvtpk(s[14], s[15])};
                    o[i] = MFMA32(__builtin_bit_cast(bf16x8, v0), __builtin_bit_cast(bf16x8, p0), o[i]);
                    o[i] = MFMA32(__builtin_bit_cast(bf16x8, v1), __builtin_bit_cast(bf16x8, p1), o[i]);
                }
            }
        }
        bool bad = false;
#pragma unroll
        for (int i = 0; i < 2; ++i) {
            float s = 0.f;
#pragma unroll
            for (int r = 0; r < 9; ++r) s += fabsf(o[i][r]);
            if (hh == 0) s += fabsf(o[i][9]) + fabsf(o[i][10]) + fabsf(o[i][11]);
            bad |= !(s < 1e30f);
        }
        if (pass == 1) break;
        if (!__syncthreads_or(bad)) break;
    }
#pragma unroll
    for (int i = 0; i < 2; ++i) {
        auto rr = __builtin_amdgcn_permlane32_swap(__float_as_uint(o[i][8]), __float_as_uint(o[i][8]), false, false);
        const float l = __uint_as_float(rr[1]);
        const float inv = 1.f / l;
        const int tok = (tb0 + i) * 32 + r32;
        char* base = ws + OIMG_OFF + (size_t)(tok >> 4) * 40960 + br * 20480 + (tok & 15) * 16;
        u32x4 v = {cvtpk(o[i][0] * inv, o[i][1] * inv), cvtpk(o[i][2] * inv, o[i][3] * inv),
                   cvtpk(o[i][4] * inv, o[i][5] * inv), cvtpk(o[i][6] * inv, o[i][7] * inv)};
        *(u32x4*)(base + (head >> 1) * 1024 + (2 * (head & 1) + hh) * 256) = v;
        if (hh == 0) {
            u32x2 w = {cvtpk(o[i][8] * inv, o[i][9] * inv), cvtpk(o[i][10] * inv, o[i][11] * inv)};
            *(u32x2*)(base + (16 + (head >> 3)) * 1024 + ((head >> 1) & 3) * 256 + (head & 1) * 8) = w;
        }
    }
}

constexpr int O_ROW = 84 * 4;
constexpr int O_PART = 64 * O_ROW;
constexpr int O_LDS = 4 * O_PART;

__global__ __launch_bounds__(256, 1) void out_kernel(const char* __restrict__ ws, unsigned short* __restrict__ out) {
    extern __shared__ __attribute__((aligned(16))) char lds[];
    const int tid = threadIdx.x, lane = tid & 63;
    const int wid = __builtin_amdgcn_readfirstlane(tid >> 6);
    const int b = blockIdx.x;
    const int xcd = b & 7, j = b >> 3;
    const int rb = xcd * 4 + (j & 3), cb = j >> 2;
    const int br = wid >> 1, ks0 = (wid & 1) * 10;
    const char* op = ws + OIMG_OFF + (size_t)(rb * 4) * 40960 + br * 20480 + ks0 * 1024 + lane * 16;
    const char* wp = ws + WOT_OFF + (size_t)(cb * 5) * 40960 + br * 20480 + ks0 * 1024 + lane * 16;
    f32x4 acc[4][5];
#pragma unroll
    for (int i = 0; i < 4; ++i)
#pragma unroll
        for (int jj = 0; jj < 5; ++jj) acc[i][jj] = f32x4{};
#pragma unroll
    for (int ks = 0; ks < 10; ++ks) {
        bf16x8 of[4], wf[5];
#pragma unroll
        for (int i = 0; i < 4; ++i) of[i] = *(const bf16x8*)(op + i * 40960 + ks * 1024);
#pragma unroll
        for (int jj = 0; jj < 5; ++jj) wf[jj] = *(const bf16x8*)(wp + jj * 40960 + ks * 1024);
#pragma unroll
        for (int i = 0; i < 4; ++i)
#pragma unroll
            for (int jj = 0; jj < 5; ++jj) acc[i][jj] = MFMA16(wf[jj], of[i], acc[i][jj]);
    }
    {
        char* pp = lds + wid * O_PART + (lane & 15) * O_ROW + (lane >> 4) * 16;
#pragma unroll
        for (int i = 0; i < 4; ++i)
#pragma unroll
            for (int jj = 0; jj < 5; ++jj) *(f32x4*)(pp + i * 16 * O_ROW + jj * 64) = acc[i][jj];
    }
    __syncthreads();
    const float* gp = (const float*)(ws + G_OFF) + rb * 64;
#pragma unroll
    for (int i = 0; i < 5; ++i) {
        const int grp = tid + 256 * i, tok = grp / 20, c4 = grp % 20;
        const char* pp = lds + tok * O_ROW + c4 * 16;
        f32x4 a = *(const f32x4*)(pp) + *(const f32x4*)(pp + O_PART);
        f32x4 bb = *(const f32x4*)(pp + 2 * O_PART) + *(const f32x4*)(pp + 3 * O_PART);
        const float g = gp[tok], omg = rbf(1.f - g);
        float r[4];
#pragma unroll
        for (int e = 0; e < 4; ++e) r[e] = rbf(g * rbf(a[e])) + rbf(omg * rbf(bb[e]));
        u32x2 v = {cvtpk(r[0], r[1]), cvtpk(r[2], r[3])};
        *(u32x2*)(out + (size_t)(rb * 64 + tok) * DM + cb * 80 + c4 * 4) = v;
    }
}

extern "C" void kernel_launch(void* const* d_in, const int* in_sizes, int n_in, void* d_out, int out_size, void* d_ws,
                              size_t ws_size, hipStream_t stream) {
    const unsigned short* x = (const unsigned short*)d_in[0];
    const unsigned short* wqkv = (const unsigned short*)d_in[1];
    const unsigned short* woa = (const unsigned short*)d_in[2];
    const unsigned short* wqb = (const unsigned short*)d_in[4];
    const unsigned short* wkb = (const unsigned short*)d_in[5];
    const unsigned short* wvb = (const unsigned short*)d_in[6];
    const unsigned short* wob = (const unsigned short*)d_in[7];
    const unsigned short* gw = (const unsigned short*)d_in[8];
    char* ws = (char*)d_ws;
    (void)hipFuncSetAttribute((const void*)proj_kernel, hipFuncAttributeMaxDynamicSharedMemorySize, P_LDS);
    (void)hipFuncSetAttribute((const void*)attn_kernel, hipFuncAttributeMaxDynamicSharedMemorySize, A_LDS);
    (void)hipFuncSetAttribute((const void*)out_kernel, hipFuncAttributeMaxDynamicSharedMemorySize, O_LDS);
    proj_kernel<<<336, 256, P_LDS, stream>>>(x, wqkv, wqb, wkb, wvb, woa, wob, gw, ws);
    attn_kernel<<<512, 256, A_LDS, stream>>>(ws);
    out_kernel<<<256, 256, O_LDS, stream>>>(ws, (unsigned short*)d_out);
}
```

```cpp
#include <hip/hip_runtime.h>
#include <hip/hip_bf16.h>
#include <stdint.h>

typedef __attribute__((ext_vector_type(8))) short bf16x8;
typedef __attribute__((ext_vector_type(4))) short s16x4;
typedef __attribute__((ext_vector_type(16))) float f32x16;
typedef __attribute__((ext_vector_type(32))) float f32x32;
typedef __attribute__((ext_vector_type(4))) float f32x4;
typedef __attribute__((ext_vector_type(4))) unsigned u32x4;
typedef __attribute__((ext_vector_type(2))) unsigned u32x2;
typedef __attribute__((address_space(3))) const char* lds_cptr;
typedef __attribute__((address_space(1))) unsigned long long gu64;
typedef __attribute__((address_space(1))) unsigned gu32;
#define DI __device__ __forceinline__
#define MFMA32(a, b, c) __builtin_amdgcn_mfma_f32_32x32x16_bf16((a), (b), (c), 0, 0, 0)
#define MFMA16(a, b, c) __builtin_amdgcn_mfma_f32_16x16x32_bf16((a), (b), (c), 0, 0, 0)
#define SBAR() __builtin_amdgcn_sched_barrier(0)
#define RLX_AGENT __ATOMIC_RELAXED, __HIP_MEMORY_SCOPE_AGENT

constexpr int DM = 640;
constexpr int QT_BYTES = 1280;
constexpr int ST = 5248;
constexpr int KVU_BYTES = 33 * ST;
constexpr unsigned QIMG_OFF = 0;
constexpr unsigned KVIMG_OFF = QIMG_OFF + 32 * 64 * QT_BYTES;
constexpr unsigned OIMG_OFF = KVIMG_OFF + 33 * KVU_BYTES;
constexpr unsigned WOT_OFF = OIMG_OFF + 128 * 40960;
constexpr unsigned G_OFF = WOT_OFF + 40 * 40960;
constexpr unsigned FLAG_OFF = G_OFF + 8192;
constexpr unsigned F_REP = 4096, F_KVB = FLAG_OFF, F_AUX = FLAG_OFF + 1024, F_PA = FLAG_OFF + 2048;
constexpr unsigned F_EPOCH = FLAG_OFF + 8 * F_REP, F_OB = F_EPOCH + 1024, F_OA = F_OB + 8192, WS_USED = F_OA + 8192;
static_assert(FLAG_OFF % 128 == 0 && KVIMG_OFF % 128 == 0 && OIMG_OFF % 128 == 0, "alignment");
constexpr float QSCALE = 0.22360679774997896f * 1.4426950408889634f;
constexpr int LDS_BYTES = 81920;
constexpr unsigned kSpinCap = 1u << 18;

DI unsigned cvtpk(float lo, float hi) {
    typedef __attribute__((ext_vector_type(2))) __bf16 bf2;
    bf2 v; v[0] = (__bf16)lo; v[1] = (__bf16)hi;
    return __builtin_bit_cast(unsigned, v);
}
DI float rbf(float x) { return (float)(__bf16)x; }
DI float bf2f(unsigned short u) { return __uint_as_float((unsigned)u << 16); }

DI void glds16(const void* g, unsigned lds_base) {
    unsigned sv;
    asm volatile("s_mov_b32 %0, m0\n\ts_mov_b32 m0, %2\n\ts_nop 0\n\tglobal_load_lds_dwordx4 %1, off\n\ts_mov_b32 m0, %0"
                 : "=&s"(sv) : "v"(g), "s"(lds_base) : "memory");
}
#define SETM0(v) asm volatile("s_mov_b32 m0, %0\n\ts_nop 0" :: "s"(v) : "memory")
#define DMA(voff, sbase, OFF) asm volatile("global_load_lds_dwordx4 %0, %1 offset:" #OFF :: "v"(voff), "s"(sbase) : "memory")
#define DMA_LO32(voff, sbase, OFF) asm volatile("s_mov_b32 exec_hi, 0\n\tglobal_load_lds_dwordx4 %0, %1 offset:" #OFF "\n\ts_mov_b32 exec_hi, -1" :: "v"(voff), "s"(sbase) : "memory")
#define WAIT_BAR(N) asm volatile("s_waitcnt vmcnt(" #N ") lgkmcnt(0)\n\ts_barrier" ::: "memory")
#define LDS_BAR() asm volatile("s_waitcnt lgkmcnt(0)\n\ts_barrier" ::: "memory")
typedef short v4i16_t __attribute__((ext_vector_type(4)));
DI s16x4 trread(lds_cptr p) {
    return __builtin_bit_cast(s16x4, __builtin_amdgcn_ds_read_tr16_b64_v4i16((__attribute__((address_space(3))) v4i16_t*)p));
}
DI bf16x8 trfrag(lds_cptr p, int four_rows) {
    s16x4 lo = trread(p), hi = trread(p + four_rows);
    return __builtin_shufflevector(lo, hi, 0, 1, 2, 3, 4, 5, 6, 7);
}
DI bf16x8 ldsb128(lds_cptr p) { return *(const __attribute__((address_space(3))) bf16x8*)p; }
typedef __amdgpu_buffer_rsrc_t rsrc_t;
DI void st16(rsrc_t r, unsigned off, u32x4 v) { __builtin_amdgcn_raw_buffer_store_b128(v, r, off, 0, 16); }
DI void st8(rsrc_t r, unsigned off, u32x2 v) { __builtin_amdgcn_raw_buffer_store_b64(v, r, off, 0, 16); }
DI void set_flag(char* ws, unsigned off, unsigned long long tag) { __hip_atomic_store((gu64*)(ws + off), tag, RLX_AGENT); }
DI void wait_flags(const char* ws, unsigned lane_off, unsigned long long tag) {
    for (unsigned spins = 0; spins < kSpinCap; ++spins) {
        const unsigned long long v = __hip_atomic_load((gu64*)(ws + lane_off), RLX_AGENT);
        if (__all(v == tag)) break;
        __builtin_amdgcn_s_sleep(6);
    }
    __builtin_amdgcn_fence(__ATOMIC_ACQUIRE, "wavefront");
}

constexpr int P_STAGE = 18432;

template <bool vtype>
DI void y_tile(const unsigned short* x, const unsigned short* wqkv, rsrc_t rs, char* lds, int rb, int cb, int rot) {
    const int lane = threadIdx.x & 63;
    const int wid = __builtin_amdgcn_readfirstlane(threadIdx.x >> 6);
    const unsigned lds0 = (unsigned)(uintptr_t)lds;
    const int r32 = lane & 31, hh = lane >> 5;
    const int tb = rb * 4 + wid;
    unsigned xo[2], wo[3];
#pragma unroll
    for (int i = 0; i < 2; ++i) {
        const int row = (wid * 2 + i) * 16 + (lane >> 2);
        xo[i] = 4096 + row * (DM * 2) + ((lane & 3) ^ ((row >> 2) & 3)) * 16 - i * 1024;
    }
#pragma unroll
    for (int i = 0; i < 3; ++i) {
        const int bb = wid * 2560 + i * 1024 + lane * 16;
        wo[i] = 4096 + (bb / 320) * 3840 + bb % 320 - i * 1024;
    }
    const char* sx = (const char*)x + (size_t)rb * (128 * DM * 2) - 4096 + rot * 64;
    const char* sw = (const char*)(wqkv + cb * 160) - 4096 + (size_t)rot * (32 * 3840);
    int left = 20 - rot;
#define Y_ADV() do { const bool wr_ = (--left == 0); sx += wr_ ? 64 - 1280 : 64; sw += wr_ ? -19 * (32 * 3840) : 32 * 3840; } while (0)
    const unsigned mx = lds0 + wid * 2048, mw = lds0 + 8192 + wid * 2560;
#pragma unroll
    for (int st = 0; st < 4; ++st) {
        SETM0(mx + st * P_STAGE); DMA(xo[0], sx, 0); DMA(xo[1], sx, 1024);
        SETM0(mw + st * P_STAGE); DMA(wo[0], sw, 0); DMA(wo[1], sw, 1024); DMA_LO32(wo[2], sw, 2048);
        Y_ADV();
    }
    const int g16 = lane >> 4, i16 = lane & 15;
    const int wbase = 8192 + (8 * (g16 >> 1) + (i16 >> 2)) * 320 + (16 * (g16 & 1) + 4 * (i16 & 3)) * 2;
    const int xb0 = (wid * 32 + r32) * 64 + ((hh ^ ((r32 >> 2) & 3)) * 16), xb1 = xb0 ^ 32;
    bf16x8 xf[2][2]; s16x4 wl[2][10], wh[2][10];
    f32x16 acc[5];
#pragma unroll
    for (int t = 0; t < 5; ++t) acc[t] = f32x16{};
#define Y_WF(B, i) __builtin_shufflevector(wl[B][i], wh[B][i], 0, 1, 2, 3, 4, 5, 6, 7)
#define Y_RD(B, i) do { wl[B][i] = trread(sb_ + wbase + ((i) / 5) * 5120 + ((i) % 5) * 64); wh[B][i] = trread(sb_ + wbase + ((i) / 5) * 5120 + ((i) % 5) * 64 + 1280); } while (0)
#define Y_STEP(B, slot_r, slot_w, LOAD) do { lds_cptr sb_ = (lds_cptr)lds + (slot_r); \
        _Pragma("unroll") for (int i = 0; i < 10; ++i) { \
            if (vtype) acc[i % 5] = MFMA32(xf[B][i / 5], Y_WF(B, i), acc[i % 5]); else acc[i % 5] = MFMA32(Y_WF(B, i), xf[B][i / 5], acc[i % 5]); \
            Y_RD(1 - (B), i); \
            if (i == 0) xf[1 - (B)][0] = ldsb128(sb_ + xb0); \
            if (i == 1) xf[1 - (B)][1] = ldsb128(sb_ + xb1); \
            if (LOAD) { if (i == 0) SETM0(mx + (slot_w)); if (i == 1) DMA(xo[0], sx, 0); if (i == 3) DMA(xo[1], sx, 1024); if (i == 4) SETM0(mw + (slot_w)); \
                        if (i == 5) DMA(wo[0], sw, 0); if (i == 7) DMA(wo[1], sw, 1024); if (i == 9) { DMA_LO32(wo[2], sw, 2048); Y_ADV(); } } \
            SBAR(); } } while (0)
    WAIT_BAR(15);
    {
        lds_cptr sb_ = (lds_cptr)lds;
        xf[0][0] = ldsb128(sb_ + xb0); xf[0][1] = ldsb128(sb_ + xb1);
#pragma unroll
        for (int i = 0; i < 10; ++i) Y_RD(0, i);
    }
    unsigned so = 0;
#pragma unroll 1
    for (int kt = 0; kt < 16; kt += 2) {
        WAIT_BAR(10); Y_STEP(0, so + P_STAGE, so, true);
        WAIT_BAR(10); Y_STEP(1, so ^ (2 * P_STAGE), so + P_STAGE, true);
        so ^= 2 * P_STAGE;
    }
#pragma unroll 1
    for (int kt = 16; kt < 20; kt += 2) {
        WAIT_BAR(0); Y_STEP(0, so + P_STAGE, 0, false);
        WAIT_BAR(0); Y_STEP(1, so ^ (2 * P_STAGE), 0, false);
        so ^= 2 * P_STAGE;
    }
    __builtin_amdgcn_s_setprio(3);
    if (vtype) {
        const unsigned tbase = KVIMG_OFF + (cb - 8) * 8 * KVU_BYTES + ((tb >> 1) + 1) * ST + 2560 + (tb & 1) * 1344;
        const unsigned base = tbase + hh * 336;
        if (hh == 0) st16(rs, tbase + (r32 >> 2) * KVU_BYTES + (r32 & 3) * 336 + 320, u32x4{0x3f803f80u, 0x3f803f80u, 0x3f803f80u, 0x3f803f80u});
#pragma unroll
        for (int t = 0; t < 5; ++t) {
            const int c = 32 * t + r32, u = c / 20, d = c % 20;
            const unsigned p = base + u * KVU_BYTES + d * 16;
#pragma unroll
            for (int s = 0; s < 2; ++s) {
                u32x4 v = {cvtpk(acc[t][8 * s], acc[t][8 * s + 1]), cvtpk(acc[t][8 * s + 2], acc[t][8 * s + 3]),
                           cvtpk(acc[t][8 * s + 4], acc[t][8 * s + 5]), cvtpk(acc[t][8 * s + 6], acc[t][8 * s + 7])};
                st16(rs, p + s * 672, v);
            }
        }
    } else {
        const bool isk = cb >= 4;
        const unsigned base = isk ? KVIMG_OFF + (cb - 4) * 8 * KVU_BYTES + (tb >> 1) * ST + (tb & 1) * 1024
                                  : QIMG_OFF + cb * 8 * (64 * QT_BYTES) + tb * QT_BYTES;
        const unsigned us = isk ? KVU_BYTES : 64 * QT_BYTES;
        const unsigned hio = isk ? 2048 - (tb & 1) * 768 : 1024;
        const float sc = isk ? 1.f : QSCALE;
        const unsigned offA = base + hh * 512 + r32 * 16, offB = base + us + (1 - hh) * 512 + r32 * 16, offC = base + hh * us + hio + r32 * 8;
#define GR(n, e) cvtpk(acc[(n) >> 2][4 * ((n) & 3) + 2 * (e)] * sc, acc[(n) >> 2][4 * ((n) & 3) + 2 * (e) + 1] * sc)
#pragma unroll
        for (int m = 0; m < 4; ++m) {
            const int n = 5 * m;
            const unsigned a0 = GR(n, 0), a1 = GR(n, 1), b0 = GR(n + 1, 0), b1 = GR(n + 1, 1), c0 = GR(n + 2, 0), c1 = GR(n + 2, 1),
                           d0 = GR(n + 3, 0), d1 = GR(n + 3, 1), e0 = GR(n + 4, 0), e1 = GR(n + 4, 1);
            st16(rs, offA + 2 * m * us, u32x4{a0, a1, b0, b1});
            st16(rs, offB + 2 * m * us, hh ? u32x4{c0, c1, d0, d1} : u32x4{d0, d1, e0, e1});
            st8(rs, offC + 2 * m * us, hh ? u32x2{e0, e1} : u32x2{c0, c1});
        }
#undef GR
    }
}

DI void y_special(const unsigned short* x, const unsigned short* wkb, const unsigned short* wvb, const unsigned short* woa,
                  const unsigned short* wob, const unsigned short* gw, char* ws, rsrc_t rs, char* lds, int tb, unsigned long long& tag) {
    const int lane = threadIdx.x & 63;
    const int wid = __builtin_amdgcn_readfirstlane(threadIdx.x >> 6);
    const unsigned lds0 = (unsigned)(uintptr_t)lds;
    const int r32 = lane & 31, hh = lane >> 5;
    const unsigned myl = lds0 + wid * 14336;
#pragma unroll
    for (int i = 0; i < 6; ++i) {
        glds16((const char*)wkb + wid * 6400 + i * 1024 + lane * 16, myl + i * 1024);
        glds16((const char*)wvb + wid * 6400 + i * 1024 + lane * 16, myl + 7168 + i * 1024);
    }
    if (lane < 16) {
        glds16((const char*)wkb + wid * 6400 + 6144 + lane * 16, myl + 6144);
        glds16((const char*)wvb + wid * 6400 + 6144 + lane * 16, myl + 7168 + 6144);
    }
    bf16x8 xf[10];
    const char* xp = (const char*)x + (size_t)(tb * 32 + r32) * (DM * 2) + wid * 320 + hh * 16;
#pragma unroll
    for (int s = 0; s < 10; ++s) asm volatile("global_load_dwordx4 %0, %1, off offset:%c2" : "=v"(xf[s]) : "v"(xp), "i"(s * 32) : "memory");
    asm volatile("s_waitcnt vmcnt(0)" : "+v"(xf[0]), "+v"(xf[1]), "+v"(xf[2]), "+v"(xf[3]), "+v"(xf[4]), "+v"(xf[5]), "+v"(xf[6]), "+v"(xf[7]), "+v"(xf[8]), "+v"(xf[9]) :: "memory");
    asm volatile("" : "+v"(tag));
    tag += 0x9E3779B97F4A7C15ull;
    const int g16 = lane >> 4, i16 = lane & 15;
    lds_cptr wp = (lds_cptr)lds + wid * 14336 + (8 * (g16 >> 1) + (i16 >> 2)) * 40 + (16 * (g16 & 1) + 4 * (i16 & 3)) * 2;
    bf16x8 kf[10], vf[10];
#pragma unroll
    for (int s = 0; s < 10; ++s) { kf[s] = trfrag(wp + s * 640, 160); vf[s] = trfrag(wp + 7168 + s * 640, 160); }
    f32x16 ak = f32x16{}, av = f32x16{};
#pragma unroll
    for (int s = 0; s < 10; ++s) { ak = MFMA32(kf[s], xf[s], ak); av = MFMA32(xf[s], vf[s], av); }
    {
        char* pp = lds + wid * 14336 + lane * 16;
#pragma unroll
        for (int g = 0; g < 3; ++g) *(f32x4*)(pp + g * 1024) = f32x4{ak[4 * g], ak[4 * g + 1], ak[4 * g + 2], ak[4 * g + 3]};
#pragma unroll
        for (int g = 0; g < 4; ++g) *(f32x4*)(pp + (3 + g) * 1024) = f32x4{av[4 * g], av[4 * g + 1], av[4 * g + 2], av[4 * g + 3]};
    }
    LDS_BAR();
    if (wid < 2) {
        const char* pp = lds + lane * 16 + wid * 3072;
        f32x4 s[4];
#pragma unroll
        for (int g = 0; g < 4; ++g)
            s[g] = (*(const f32x4*)(pp + g * 1024) + *(const f32x4*)(pp + 14336 + g * 1024)) + (*(const f32x4*)(pp + 2 * 14336 + g * 1024) + *(const f32x4*)(pp + 3 * 14336 + g * 1024));
        const u32x4 v0 = {cvtpk(s[0][0], s[0][1]), cvtpk(s[0][2], s[0][3]), cvtpk(s[1][0], s[1][1]), cvtpk(s[1][2], s[1][3])};
        const u32x4 v1 = {cvtpk(s[2][0], s[2][1]), cvtpk(s[2][2], s[2][3]), cvtpk(s[3][0], s[3][1]), cvtpk(s[3][2], s[3][3])};
        if (wid == 0) {
            const unsigned base = KVIMG_OFF + 32 * KVU_BYTES + (tb >> 1) * ST;
            st16(rs, base + (tb & 1) * 1024 + lane * 16, v0);
            if (hh == 0) st8(rs, base + 2048 + (tb & 1) * 256 + r32 * 8, u32x2{v1[0], v1[1]});
        } else if (r32 <= 20) {
            const unsigned base = KVIMG_OFF + 32 * KVU_BYTES + ((tb >> 1) + 1) * ST + 2560 + (tb & 1) * 1344 + hh * 336 + r32 * 16;
            const u32x4 ones = {0x3f803f80u, 0x3f803f80u, 0x3f803f80u, 0x3f803f80u};
            st16(rs, base, r32 == 20 ? ones : v0);
            st16(rs, base + 672, r32 == 20 ? ones : v1);
        }
        asm volatile("s_waitcnt vmcnt(0)" ::: "memory");
        if (lane < 8) set_flag(ws, F_KVB + lane * F_REP + (wid * 64 + tb) * 8, tag);
    }
    {
        const int sub = lane & 15;
        bf16x8 gg[5], gx[2][5];
#pragma unroll
        for (int i = 0; i < 5; ++i) {
            const int k = (i * 16 + sub) * 8;
            gg[i] = *(const bf16x8*)(gw + k);
#pragma unroll
            for (int ps = 0; ps < 2; ++ps) gx[ps][i] = *(const bf16x8*)(x + (size_t)(tb * 32 + wid * 8 + ps * 4 + (lane >> 4)) * DM + k);
        }
#pragma unroll
        for (int ps = 0; ps < 2; ++ps) {
            float z = 0.f;
#pragma unroll
            for (int i = 0; i < 5; ++i)
#pragma unroll
                for (int e = 0; e < 8; ++e) z += rbf(bf2f((unsigned short)gx[ps][i][e]) * bf2f((unsigned short)gg[i][e]));
#pragma unroll
            for (int o = 8; o >= 1; o >>= 1) z += __shfl_xor(z, o);
            z = rbf(z);
            const float g = rbf(1.f / (1.f + __expf(-z)));
            if (sub == 0) __hip_atomic_store((gu32*)(ws + G_OFF + (tb * 32 + wid * 8 + ps * 4 + (lane >> 4)) * 4), __float_as_uint(g), RLX_AGENT);
        }
    }
    {
        unsigned short we[7][8];
#pragma unroll
        for (int q = 0; q < 7; ++q) {
            const int i = (q * 4 + wid < 25) ? q * 4 + wid : wid;
            const int f = tb * 25 + i;
            const int ks = f % 20, br = (f / 20) & 1, cb16 = f / 40;
            const int kq = lane >> 4;
            const int ra = (ks < 16) ? (2 * ks + (kq >> 1)) * 20 + 4 * (kq & 1) : (8 * (ks - 16) + 2 * kq) * 20 + 16;
            const unsigned short* pa = (br ? wob : woa) + cb16 * 16 + (lane & 15) + ra * DM;
            const unsigned short* pb = pa + ((ks < 16) ? 8 : 20) * DM;
#pragma unroll
            for (int jj = 0; jj < 4; ++jj) { we[q][jj] = pa[jj * DM]; we[q][4 + jj] = pb[jj * DM]; }
        }
#pragma unroll
        for (int q = 0; q < 7; ++q) {
            const int i = (q * 4 + wid < 25) ? q * 4 + wid : wid;
            u32x4 v = {(unsigned)we[q][0] | ((unsigned)we[q][1] << 16), (unsigned)we[q][2] | ((unsigned)we[q][3] << 16),
                       (unsigned)we[q][4] | ((unsigned)we[q][5] << 16), (unsigned)we[q][6] | ((unsigned)we[q][7] << 16)};
            st16(rs, WOT_OFF + (tb * 25 + i) * 1024 + lane * 16, v);
        }
    }
}

constexpr int A_CHUNK = 4 * ST, A_SLOT0 = 36864;
DI void kv_start(const char* kvbase, unsigned lds0, bf16x8& k0x, bf16x8& k0y, s16x4& k1) {
    const int lane = threadIdx.x & 63;
    const int wid = __builtin_amdgcn_readfirstlane(threadIdx.x >> 6);
    asm volatile("global_load_dwordx4 %0, %3, %5 sc1\n\t"
                 "global_load_dwordx4 %1, %3, %5 offset:1024 sc1\n\t"
                 "global_load_dwordx2 %2, %4, %5 offset:2048 sc1"
                 : "=&v"(k0x), "=&v"(k0y), "=&v"(k1) : "v"(lane * 16), "v"(lane * 8), "s"(kvbase) : "memory");
    const unsigned vo = wid * ST + lane * 16;
#pragma unroll
    for (int c = 0; c < 2; ++c) {
        const char* src = kvbase + ST + c * A_CHUNK;
        SETM0(lds0 + A_SLOT0 + c * A_CHUNK + wid * ST);
        asm volatile("global_load_lds_dwordx4 %0, %1 sc1\n\tglobal_load_lds_dwordx4 %0, %1 offset:1024 sc1\n\t"
                     "global_load_lds_dwordx4 %0, %1 offset:2048 sc1\n\tglobal_load_lds_dwordx4 %0, %1 offset:3072 sc1" :: "v"(vo), "s"(src) : "memory");
        SETM0(lds0 + A_SLOT0 + c * A_CHUNK + wid * ST + 4096);
        asm volatile("global_load_lds_dwordx4 %0, %1 sc1\n\ts_mov_b64 exec, 0xff\n\tglobal_load_lds_dwordx4 %0, %1 offset:1024 sc1\n\ts_mov_b64 exec, -1"
                     :: "v"(vo + 4096), "s"(src) : "memory");
    }
}

constexpr int XQ_STAGE = 18432;
DI void x_qproj(const unsigned short* x, const unsigned short* wqb, char* lds, int rb64, int hg, int rot, bf16x8 (&q0)[2], s16x4 (&q1)[2],
                bf16x8& k0x, bf16x8& k0y, s16x4& k1, char* ws, unsigned kvb_flags, unsigned long long& tag) {
    const int lane = threadIdx.x & 63;
    const int wid = __builtin_amdgcn_readfirstlane(threadIdx.x >> 6);
    const unsigned lds0 = (unsigned)(uintptr_t)lds;
    const int r32 = lane & 31, hh = lane >> 5;
    unsigned xo[2], wo[3];
#pragma unroll
    for (int i = 0; i < 2; ++i) {
        const int row = (wid * 2 + i) * 8 + (lane >> 3);
        xo[i] = 4096 + row * (DM * 2) + ((lane & 7) ^ ((row >> 1) & 7)) * 16 - i * 1024;
    }
#pragma unroll
    for (int i = 0; i < 3; ++i) {
        const int bb = wid * 2560 + i * 1024 + lane * 16;
        wo[i] = 4096 + (bb / 160) * 1280 + bb % 160 - i * 1024;
    }
    const char* sx = (const char*)x + (size_t)rb64 * (64 * DM * 2) - 4096 + rot * 128;
    const char* sw = (const char*)(wqb + hg * 80) - 4096 + (size_t)rot * (64 * 1280);
    int left = 10 - rot;
#define X_ADV() do { const bool wr_ = (--left == 0); sx += wr_ ? 128 - 1280 : 128; sw += wr_ ? -9 * (64 * 1280) : 64 * 1280; } while (0)
    const unsigned mx = lds0 + wid * 2048, mw = lds0 + 8192 + wid * 2560;
#pragma unroll
    for (int st = 0; st < 4; ++st) {
        SETM0(mx + st * XQ_STAGE); DMA(xo[0], sx, 0); DMA(xo[1], sx, 1024);
        SETM0(mw + st * XQ_STAGE); DMA(wo[0], sw, 0); DMA(wo[1], sw, 1024); DMA_LO32(wo[2], sw, 2048);
        X_ADV();
    }
    const int g16 = lane >> 4, i16 = lane & 15;
    const int wbase = 8192 + (8 * (g16 >> 1) + (i16 >> 2)) * 160 + (20 * wid + 16 * (g16 & 1) + 4 * (i16 & 3)) * 2;
    const int xsw = (r32 >> 1) & 7;
    int xb[4];
#pragma unroll
    for (int s = 0; s < 4; ++s) xb[s] = r32 * 128 + (((2 * s + hh) ^ xsw) * 16);
    bf16x8 xf[2][8]; s16x4 wl[2][4], wh[2][4];
    f32x16 acc[2] = {f32x16{}, f32x16{}};
#define X_RD(B, i) do { xf[B][i] = ldsb128(sb_ + xb[(i) >> 1] + ((i) & 1) * 4096); \
        if ((i) & 1) wh[B][(i) >> 1] = trread(sb_ + wbase + ((i) >> 1) * 2560 + 640); else wl[B][(i) >> 1] = trread(sb_ + wbase + ((i) >> 1) * 2560); } while (0)
#define X_STEP(B, slot_r, slot_w, LOAD) do { lds_cptr sb_ = (lds_cptr)lds + (slot_r); \
        _Pragma("unroll") for (int i = 0; i < 8; ++i) { \
            acc[i & 1] = MFMA32(__builtin_shufflevector(wl[B][i >> 1], wh[B][i >> 1], 0, 1, 2, 3, 4, 5, 6, 7), xf[B][i], acc[i & 1]); \
            X_RD(1 - (B), i); \
            if (LOAD) { if (i == 0) SETM0(mx + (slot_w)); if (i == 1) DMA(xo[0], sx, 0); if (i == 2) DMA(xo[1], sx, 1024); if (i == 3) SETM0(mw + (slot_w)); \
                        if (i == 4) DMA(wo[0], sw, 0); if (i == 5) DMA(wo[1], sw, 1024); if (i == 7) { DMA_LO32(wo[2], sw, 2048); X_ADV(); } } \
            SBAR(); } } while (0)
    WAIT_BAR(15);
    {
        lds_cptr sb_ = (lds_cptr)lds;
#pragma unroll
        for (int i = 0; i < 8; ++i) X_RD(0, i);
    }
    unsigned so = 0;
#pragma unroll 1
    for (int kt = 0; kt < 6; kt += 2) {
        WAIT_BAR(10); X_STEP(0, so + XQ_STAGE, so, true);
        WAIT_BAR(10); X_STEP(1, so ^ (2 * XQ_STAGE), so + XQ_STAGE, true);
        so ^= 2 * XQ_STAGE;
    }
    WAIT_BAR(10); X_STEP(0, so + XQ_STAGE, 0, false);
    WAIT_BAR(5); X_STEP(1, so ^ (2 * XQ_STAGE), 0, false);
    so ^= 2 * XQ_STAGE;
    asm volatile("" : "+v"(tag));
    tag += 0x9E3779B97F4A7C15ull;
    if (wid < 2) wait_flags(ws, kvb_flags + wid * 512 + lane * 8, tag);
    WAIT_BAR(0);
    kv_start(ws + KVIMG_OFF + 32 * KVU_BYTES, lds0, k0x, k0y, k1);
    X_STEP(0, so + XQ_STAGE, 0, false);
    X_STEP(1, so, 0, false);
#pragma unroll
    for (int i = 0; i < 2; ++i) {
        u32x4 v = {cvtpk(acc[i][0] * QSCALE, acc[i][1] * QSCALE), cvtpk(acc[i][2] * QSCALE, acc[i][3] * QSCALE),
                   cvtpk(acc[i][4] * QSCALE, acc[i][5] * QSCALE), cvtpk(acc[i][6] * QSCALE, acc[i][7] * QSCALE)};
        q0[i] = __builtin_bit_cast(bf16x8, v);
        const unsigned a = cvtpk(acc[i][8] * QSCALE, acc[i][9] * QSCALE), b = cvtpk(acc[i][10] * QSCALE, acc[i][11] * QSCALE);
        auto ra = __builtin_amdgcn_permlane32_swap(a, a, false, false);
        auto rb = __builtin_amdgcn_permlane32_swap(b, b, false, false);
        q1[i] = __builtin_bit_cast(s16x4, u32x2{ra[0], rb[0]});
    }
    asm volatile("s_waitcnt vmcnt(12)" : "+v"(k0x), "+v"(k0y), "+v"(k1));
}


DI float xhalf_max(float m) {
    auto rr = __builtin_amdgcn_permlane32_swap(__float_as_uint(m), __float_as_uint(m), false, false);
    return fmaxf(__uint_as_float(rr[0]), __uint_as_float(rr[1]));
}
DI f32x32 pair_scores(bf16x8 k0x, bf16x8 k0y, s16x4 k1, bf16x8 q0, s16x4 q1) {
    f32x16 s0 = MFMA32(k0x, q0, f32x16{});
    f32x16 s1 = MFMA32(k0y, q0, f32x16{});
    f32x32 s = __builtin_shufflevector(s0, s1, 0, 1, 2, 3, 4, 5, 6, 7, 8, 9, 10, 11, 12, 13, 14, 15, 16, 17, 18, 19, 20, 21, 22, 23, 24, 25, 26, 27, 28, 29, 30, 31);
    return __builtin_amdgcn_mfma_f32_32x32x4bf16_1k(k1, q1, s, 0, 0, 0);
}
DI float pair_max(bf16x8 k0x, bf16x8 k0y, s16x4 k1, bf16x8 q0, s16x4 q1) {
    f32x32 s = pair_scores(k0x, k0y, k1, q0, q1);
    float a = fmaxf(s[0], s[1]);
#pragma unroll
    for (int r = 2; r < 32; r += 2) a = fmaxf(fmaxf(a, s[r]), s[r + 1]);
    return a;
}
DI void attn_slow(const char* kvbase, const bf16x8 (&q0)[2], const s16x4 (&q1)[2], f32x16 (&o)[2]) {
    const int lane = threadIdx.x & 63, r32 = lane & 31, hh = lane >> 5;
    __builtin_amdgcn_fence(__ATOMIC_ACQUIRE, "agent");
    float m[2] = {-INFINITY, -INFINITY};
    for (int p = 0; p < 32; ++p) {
        const char* kp = kvbase + p * ST;
        bf16x8 ax = *(const bf16x8*)(kp + lane * 16);
        bf16x8 ay = *(const bf16x8*)(kp + 1024 + lane * 16);
        s16x4 a1 = *(const s16x4*)(kp + 2048 + lane * 8);
        m[0] = fmaxf(m[0], pair_max(ax, ay, a1, q0[0], q1[0]));
        m[1] = fmaxf(m[1], pair_max(ax, ay, a1, q0[1], q1[1]));
    }
    m[0] = xhalf_max(m[0]); m[1] = xhalf_max(m[1]);
    o[0] = f32x16{}; o[1] = f32x16{};
    for (int p = 0; p < 32; ++p) {
        const char* kp = kvbase + p * ST;
        bf16x8 ax = *(const bf16x8*)(kp + lane * 16);
        bf16x8 ay = *(const bf16x8*)(kp + 1024 + lane * 16);
        s16x4 a1 = *(const s16x4*)(kp + 2048 + lane * 8);
        u32x4 vf[4];
#pragma unroll
        for (int f = 0; f < 4; ++f) {
            vf[f] = *(const u32x4*)(kp + ST + 2560 + f * 672 + hh * 336 + (r32 < 20 ? r32 : 20) * 16);
        }
#pragma unroll
        for (int i = 0; i < 2; ++i) {
            f32x32 s = pair_scores(ax, ay, a1, q0[i], q1[i]);
#pragma unroll
            for (int r = 0; r < 32; ++r) s[r] = __builtin_amdgcn_exp2f(s[r] - m[i]);
#pragma unroll
            for (int f = 0; f < 4; ++f) {
                u32x4 pp = {cvtpk(s[8 * f], s[8 * f + 1]), cvtpk(s[8 * f + 2], s[8 * f + 3]), cvtpk(s[8 * f + 4], s[8 * f + 5]), cvtpk(s[8 * f + 6], s[8 * f + 7])};
                o[i] = MFMA32(__builtin_bit_cast(bf16x8, vf[f]), __builtin_bit_cast(bf16x8, pp), o[i]);
            }
        }
    }
}

constexpr int O_STRIP = 5 * 1024;
#define KSOFF(ks) ((ks) < 4 ? (ks) * 1024 : 0)
DI void out_load_w(const char* wp, const char* wp2, unsigned l16, bf16x8 (&wf)[5][5]) {
#pragma unroll
    for (int ks = 0; ks < 5; ++ks)
#pragma unroll
        for (int jj = 0; jj < 5; ++jj)
            asm volatile("global_load_dwordx4 %0, %1, %2 offset:%c3 sc1" : "=v"(wf[ks][jj]) : "v"(l16), "s"((ks < 4 ? wp : wp2) + jj * 40960), "i"(KSOFF(ks)) : "memory");
}
__global__ __launch_bounds__(256, 2) void fused_kernel(
    const unsigned short* __restrict__ x, const unsigned short* __restrict__ wqkv, const unsigned short* __restrict__ wqb,
    const unsigned short* __restrict__ wkb, const unsigned short* __restrict__ wvb, const unsigned short* __restrict__ woa,
    const unsigned short* __restrict__ wob, const unsigned short* __restrict__ gw, char* __restrict__ ws, unsigned short* __restrict__ out) {
    extern __shared__ __attribute__((aligned(16))) char lds[];
    const int tid = threadIdx.x, lane = tid & 63;
    const int wid = __builtin_amdgcn_readfirstlane(tid >> 6);
    const int r32 = lane & 31, hh = lane >> 5;
    const int b = blockIdx.x;
    const bool isX = b < 256;
    const int xcd = b & 7, j = (b >> 3) & 31;
    const unsigned lds0 = (unsigned)(uintptr_t)lds;
    const rsrc_t rs = __builtin_amdgcn_make_buffer_rsrc(ws, 0, WS_USED, 0x00020000);
    unsigned long long tag = __hip_atomic_load((gu64*)(ws + F_EPOCH), RLX_AGENT);
    unsigned warm = 0;
    if (wid == 3) {
        unsigned long long pc; unsigned len;
        asm volatile("s_getpc_b64 %0\n.Lfk_pc0:\n\ts_mov_b32 %1, .Lfk_end-.Lfk_pc0" : "=s"(pc), "=s"(len));
        const unsigned co = ((b >> 3) * 64 + lane) * 128;
        asm volatile("global_load_dword %0, %1, %2" : "=v"(warm) : "v"(co < len ? co : 0u), "s"(pc) : "memory");
    }
#define MAKE_TAG() do { asm volatile("" : "+v"(tag)); tag += 0x9E3779B97F4A7C15ull; } while (0)

    bf16x8 q0[2], k0x, k0y; s16x4 q1[2], k1;
    const int rb64 = xcd + 8 * (j & 3), hg = j >> 2;
    const int qb = j & 7;
    const int head = isX ? hg * 4 + wid : (j >> 3) * 8 + xcd;
    const int tb0 = isX ? rb64 * 2 : qb * 8 + wid * 2;
    const unsigned obase = OIMG_OFF + (tb0 * 2 + (r32 >> 4)) * 40960 + (isX ? 20480 : 0) + (r32 & 15) * 16;
    const unsigned om = obase + (head >> 1) * 1024 + (2 * (head & 1) + hh) * 256;
    const unsigned orem = obase + hh * 81920 + (16 + (head >> 3)) * 1024 + ((head >> 1) & 3) * 256 + (head & 1) * 8;
#define MARK_O() do { st16(rs, om, u32x4{~0u, ~0u, ~0u, ~0u}); st16(rs, om + 81920, u32x4{~0u, ~0u, ~0u, ~0u}); st8(rs, orem, u32x2{~0u, ~0u}); } while (0)
    if (isX) {
        __builtin_amdgcn_s_setprio(1);
        x_qproj(x, wqb, lds, rb64, hg, (((j & 3) * 5) / 2 + hg) % 10, q0, q1, k0x, k0y, k1, ws, F_KVB + xcd * F_REP, tag);
        __builtin_amdgcn_s_setprio(3);
    } else {
        unsigned fl;
        if (j < 24) {
            const int rb = (xcd & 1) * 8 + (j & 7), cb = (xcd >> 1) * 3 + (j >> 3);
            __builtin_amdgcn_s_setprio(0);
            const int rot = ((j & 7) * 5 / 2 + (j >> 3) * 7) % 20;
            if (cb >= 8) y_tile<true>(x, wqkv, rs, lds, rb, cb, rot); else y_tile<false>(x, wqkv, rs, lds, rb, cb, rot);
            MAKE_TAG();
            fl = F_PA + (cb * 16 + rb) * 8;
        } else {
            __builtin_amdgcn_s_setprio(3);
            const int tb = (j - 24) * 8 + xcd;
            y_special(x, wkb, wvb, woa, wob, gw, ws, rs, lds, tb, tag);
            fl = F_AUX + tb * 8;
        }
        WAIT_BAR(0);
        if (tid < 8) set_flag(ws, fl + tid * F_REP, tag);
        if (wid == 0) {
            const int hc = head >> 3;
            const int idx = (lane < 16) ? (4 + hc) * 16 + lane : (lane < 32) ? (8 + hc) * 16 + (lane - 16) : hc * 16 + qb * 2 + (lane & 1);
            wait_flags(ws, F_PA + xcd * F_REP + idx * 8, tag);
        }
        __builtin_amdgcn_s_setprio(3);
        LDS_BAR();
        const char* qp = ws + QIMG_OFF + ((size_t)head * 64 + tb0) * QT_BYTES;
        asm volatile("global_load_dwordx4 %0, %4, %6 sc1\n\t"
                     "global_load_dwordx2 %2, %5, %6 offset:1024 sc1\n\t"
                     "global_load_dwordx4 %1, %4, %6 offset:1280 sc1\n\t"
                     "global_load_dwordx2 %3, %5, %6 offset:2304 sc1"
                     : "=&v"(q0[0]), "=&v"(q0[1]), "=&v"(q1[0]), "=&v"(q1[1]) : "v"(lane * 16), "v"(r32 * 8), "s"(qp) : "memory");
        kv_start(ws + KVIMG_OFF + (size_t)head * KVU_BYTES, lds0, k0x, k0y, k1);
        asm volatile("s_waitcnt vmcnt(12)" : "+v"(k0x), "+v"(k0y), "+v"(k1), "+v"(q0[0]), "+v"(q0[1]), "+v"(q1[0]), "+v"(q1[1]));
    }
    asm volatile("" :: "v"(warm));

    const char* kvbase = ws + KVIMG_OFF + (size_t)(isX ? 32 : head) * KVU_BYTES;
    const unsigned voff = wid * ST + lane * 16;
    f32x16 o[2];
    {
        f32x16 ca, cb;
        {
            const float ma = -xhalf_max(pair_max(k0x, k0y, k1, q0[0], q1[0])), mb = -xhalf_max(pair_max(k0x, k0y, k1, q0[1], q1[1]));
#pragma unroll
            for (int r = 0; r < 16; ++r) { ca[r] = ma; cb[r] = mb; }
        }
        o[0] = f32x16{}; o[1] = f32x16{};
        u32x4 v0 = {0u, 0u, 0u, 0u}, v1 = v0, v2 = v0, v3 = v0;
        unsigned aK = lds0 + A_SLOT0 + lane * 16, aK1 = lds0 + A_SLOT0 + lane * 8, aV = lds0 + A_SLOT0 + hh * 336 + (r32 < 20 ? r32 : 20) * 16;
        MARK_O();
        if (isX) __builtin_amdgcn_s_setprio(2); else __builtin_amdgcn_s_setprio(1);
        asm volatile(
        "s_nop 1\n\t"
        "s_mov_b32 s46, m0\n\t"
        "s_mov_b64 s[40:41], %20\n\t"
        "s_mov_b32 s45, %21\n\t"
        "s_add_u32 s48, s45, 57856\n\t"
        "s_add_u32 s47, s45, 36864\n\t"
        "s_mov_b32 s44, 8\n\t"
        "v_mfma_f32_32x32x16_bf16 v[64:79], %2, %14, %12\n\t"
        "v_mfma_f32_32x32x16_bf16 v[80:95], %3, %14, %12\n\t"
        "v_mov_b32 v144, 0\n\t"
        "v_mov_b32 v145, 0\n\t"
        "v_mov_b32 v146, 0\n\t"
        "v_mov_b32 v147, 0\n\t"
        "v_mov_b32 v148, 0\n\t"
        "v_mov_b32 v149, 0\n\t"
        "v_mov_b32 v150, 0\n\t"
        "v_mov_b32 v151, 0\n\t"
        "v_mov_b32 v152, 0\n\t"
        "v_mov_b32 v153, 0\n\t"
        "v_mov_b32 v154, 0\n\t"
        "v_mov_b32 v155, 0\n\t"
        "v_mov_b32 v156, 0\n\t"
        "v_mov_b32 v157, 0\n\t"
        "v_mov_b32 v158, 0\n\t"
        "v_mov_b32 v159, 0\n\t"
        "v_mfma_f32_32x32x4_2b_bf16 v[64:95], %4, %16, v[64:95]\n\t"
        "s_waitcnt vmcnt(9)\n\t"
        "s_barrier\n\t"
        "s_nop 15\n\t"
        "s_nop 7\n\t"
        "0:\n\t"
        "v_mfma_f32_32x32x16_bf16 v[96:111], %2, %15, %13\n\t"
        "ds_read_b128 %2, %9 offset:0\n\t"
        "v_exp_f32 v64, v64\n\t"
        "v_exp_f32 v65, v65\n\t"
        "v_exp_f32 v66, v66\n\t"
        "v_exp_f32 v67, v67\n\t"
        "v_cvt_pk_bf16_f32 v128, v64, v65\n\t"
        "v_cvt_pk_bf16_f32 v129, v66, v67\n\t"
        "v_mfma_f32_32x32x16_bf16 v[112:127], %3, %15, %13\n\t"
        "ds_read_b128 %3, %9 offset:1024\n\t"
        "v_exp_f32 v68, v68\n\t"
        "v_exp_f32 v69, v69\n\t"
        "v_exp_f32 v70, v70\n\t"
        "v_exp_f32 v71, v71\n\t"
        "v_cvt_pk_bf16_f32 v130, v68, v69\n\t"
        "v_cvt_pk_bf16_f32 v131, v70, v71\n\t"
        "v_mfma_f32_32x32x16_bf16 %1, %5, v[144:147], %1\n\t"
        "ds_read_b128 %5, %11 offset:2560\n\t"
        "v_exp_f32 v72, v72\n\t"
        "v_exp_f32 v73, v73\n\t"
        "v_exp_f32 v74, v74\n\t"
        "v_exp_f32 v75, v75\n\t"
        "v_cvt_pk_bf16_f32 v132, v72, v73\n\t"
        "v_cvt_pk_bf16_f32 v133, v74, v75\n\t"
        "v_mfma_f32_32x32x16_bf16 %1, %6, v[148:151], %1\n\t"
        "ds_read_b128 %6, %11 offset:3232\n\t"
        "v_exp_f32 v76, v76\n\t"
        "v_exp_f32 v77, v77\n\t"
        "v_exp_f32 v78, v78\n\t"
        "v_exp_f32 v79, v79\n\t"
        "v_cvt_pk_bf16_f32 v134, v76, v77\n\t"
        "v_cvt_pk_bf16_f32 v135, v78, v79\n\t"
        "v_mfma_f32_32x32x4_2b_bf16 v[96:127], %4, %17, v[96:127]\n\t"
        "s_sub_u32 s44, s44, 1\n\t"
        "v_exp_f32 v80, v80\n\t"
        "v_exp_f32 v81, v81\n\t"
        "v_exp_f32 v82, v82\n\t"
        "v_exp_f32 v83, v83\n\t"
        "v_cvt_pk_bf16_f32 v136, v80, v81\n\t"
        "v_cvt_pk_bf16_f32 v137, v82, v83\n\t"
        "v_exp_f32 v84, v84\n\t"
        "v_exp_f32 v85, v85\n\t"
        "v_exp_f32 v86, v86\n\t"
        "v_exp_f32 v87, v87\n\t"
        "v_cvt_pk_bf16_f32 v138, v84, v85\n\t"
        "v_cvt_pk_bf16_f32 v139, v86, v87\n\t"
        "v_mfma_f32_32x32x16_bf16 %1, %7, v[152:155], %1\n\t"
        "ds_read_b128 %7, %11 offset:3904\n\t"
        "v_exp_f32 v88, v88\n\t"
        "v_exp_f32 v89, v89\n\t"
        "v_exp_f32 v90, v90\n\t"
        "v_exp_f32 v91, v91\n\t"
        "v_cvt_pk_bf16_f32 v140, v88, v89\n\t"
        "v_cvt_pk_bf16_f32 v141, v90, v91\n\t"
        "v_mfma_f32_32x32x16_bf16 %1, %8, v[156:159], %1\n\t"
        "s_waitcnt lgkmcnt(1)\n\t"
        "v_exp_f32 v92, v92\n\t"
        "v_exp_f32 v93, v93\n\t"
        "v_exp_f32 v94, v94\n\t"
        "v_exp_f32 v95, v95\n\t"
        "v_cvt_pk_bf16_f32 v142, v92, v93\n\t"
        "v_cvt_pk_bf16_f32 v143, v94, v95\n\t"
        "v_mfma_f32_32x32x16_bf16 v[64:79], %2, %14, %12\n\t"
        "ds_read_b128 %8, %11 offset:4576\n\t"
        "v_exp_f32 v96, v96\n\t"
        "v_exp_f32 v97, v97\n\t"
        "v_exp_f32 v98, v98\n\t"
        "v_exp_f32 v99, v99\n\t"
        "v_cvt_pk_bf16_f32 v144, v96, v97\n\t"
        "v_cvt_pk_bf16_f32 v145, v98, v99\n\t"
        "v_mfma_f32_32x32x16_bf16 v[80:95], %3, %14, %12\n\t"
        "ds_read_b64 %4, %10 offset:2048\n\t"
        "v_exp_f32 v100, v100\n\t"
        "v_exp_f32 v101, v101\n\t"
        "v_exp_f32 v102, v102\n\t"
        "v_exp_f32 v103, v103\n\t"
        "v_cvt_pk_bf16_f32 v146, v100, v101\n\t"
        "v_cvt_pk_bf16_f32 v147, v102, v103\n\t"
        "v_mfma_f32_32x32x16_bf16 %0, %5, v[128:131], %0\n\t"
        "s_mov_b32 m0, s45\n\t"
        "v_exp_f32 v104, v104\n\t"
        "v_exp_f32 v105, v105\n\t"
        "v_exp_f32 v106, v106\n\t"
        "v_exp_f32 v107, v107\n\t"
        "v_cvt_pk_bf16_f32 v148, v104, v105\n\t"
        "v_cvt_pk_bf16_f32 v149, v106, v107\n\t"
        "v_mfma_f32_32x32x16_bf16 %0, %6, v[132:135], %0\n\t"
        "s_waitcnt lgkmcnt(0)\n\t"
        "v_exp_f32 v108, v108\n\t"
        "v_exp_f32 v109, v109\n\t"
        "v_exp_f32 v110, v110\n\t"
        "v_exp_f32 v111, v111\n\t"
        "v_cvt_pk_bf16_f32 v150, v108, v109\n\t"
        "v_cvt_pk_bf16_f32 v151, v110, v111\n\t"
        "v_mfma_f32_32x32x4_2b_bf16 v[64:95], %4, %16, v[64:95]\n\t"
        "s_cmp_gt_u32 s44, 2\n\t"
        "v_exp_f32 v112, v112\n\t"
        "v_exp_f32 v113, v113\n\t"
        "v_exp_f32 v114, v114\n\t"
        "v_exp_f32 v115, v115\n\t"
        "v_cvt_pk_bf16_f32 v152, v112, v113\n\t"
        "v_cvt_pk_bf16_f32 v153, v114, v115\n\t"
        "v_exp_f32 v116, v116\n\t"
        "v_exp_f32 v117, v117\n\t"
        "v_exp_f32 v118, v118\n\t"
        "v_exp_f32 v119, v119\n\t"
        "v_cvt_pk_bf16_f32 v154, v116, v117\n\t"
        "v_cvt_pk_bf16_f32 v155, v118, v119\n\t"
        "v_mfma_f32_32x32x16_bf16 %0, %7, v[136:139], %0\n\t"
        "global_load_lds_dwordx4 %18, s[40:41] offset:0 sc1\n\t"
        "v_exp_f32 v120, v120\n\t"
        "v_exp_f32 v121, v121\n\t"
        "v_exp_f32 v122, v122\n\t"
        "v_exp_f32 v123, v123\n\t"
        "v_cvt_pk_bf16_f32 v156, v120, v121\n\t"
        "v_cvt_pk_bf16_f32 v157, v122, v123\n\t"
        "v_mfma_f32_32x32x16_bf16 %0, %8, v[140:143], %0\n\t"
        "global_load_lds_dwordx4 %18, s[40:41] offset:1024 sc1\n\t"
        "v_exp_f32 v124, v124\n\t"
        "v_exp_f32 v125, v125\n\t"
        "v_exp_f32 v126, v126\n\t"
        "v_exp_f32 v127, v127\n\t"
        "v_cvt_pk_bf16_f32 v158, v124, v125\n\t"
        "v_cvt_pk_bf16_f32 v159, v126, v127\n\t"
        "v_mfma_f32_32x32x16_bf16 v[96:111], %2, %15, %13\n\t"
        "ds_read_b128 %2, %9 offset:5248\n\t"
        "v_exp_f32 v64, v64\n\t"
        "v_exp_f32 v65, v65\n\t"
        "v_exp_f32 v66, v66\n\t"
        "v_exp_f32 v67, v67\n\t"
        "v_cvt_pk_bf16_f32 v128, v64, v65\n\t"
        "v_cvt_pk_bf16_f32 v129, v66, v67\n\t"
        "v_mfma_f32_32x32x16_bf16 v[112:127], %3, %15, %13\n\t"
        "ds_read_b128 %3, %9 offset:6272\n\t"
        "v_exp_f32 v68, v68\n\t"
        "v_exp_f32 v69, v69\n\t"
        "v_exp_f32 v70, v70\n\t"
        "v_exp_f32 v71, v71\n\t"
        "v_cvt_pk_bf16_f32 v130, v68, v69\n\t"
        "v_cvt_pk_bf16_f32 v131, v70, v71\n\t"
        "v_mfma_f32_32x32x16_bf16 %1, %5, v[144:147], %1\n\t"
        "ds_read_b128 %5, %11 offset:7808\n\t"
        "v_exp_f32 v72, v72\n\t"
        "v_exp_f32 v73, v73\n\t"
        "v_exp_f32 v74, v74\n\t"
        "v_exp_f32 v75, v75\n\t"
        "v_cvt_pk_bf16_f32 v132, v72, v73\n\t"
        "v_cvt_pk_bf16_f32 v133, v74, v75\n\t"
        "v_mfma_f32_32x32x16_bf16 %1, %6, v[148:151], %1\n\t"
        "ds_read_b128 %6, %11 offset:8480\n\t"
        "v_exp_f32 v76, v76\n\t"
        "v_exp_f32 v77, v77\n\t"
        "v_exp_f32 v78, v78\n\t"
        "v_exp_f32 v79, v79\n\t"
        "v_cvt_pk_bf16_f32 v134, v76, v77\n\t"
        "v_cvt_pk_bf16_f32 v135, v78, v79\n\t"
        "v_mfma_f32_32x32x4_2b_bf16 v[96:127], %4, %17, v[96:127]\n\t"
        "s_cselect_b32 s43, 20992, 0\n\t"
        "v_exp_f32 v80, v80\n\t"
        "v_exp_f32 v81, v81\n\t"
        "v_exp_f32 v82, v82\n\t"
        "v_exp_f32 v83, v83\n\t"
        "v_cvt_pk_bf16_f32 v136, v80, v81\n\t"
        "v_cvt_pk_bf16_f32 v137, v82, v83\n\t"
        "v_exp_f32 v84, v84\n\t"
        "v_exp_f32 v85, v85\n\t"
        "v_exp_f32 v86, v86\n\t"
        "v_exp_f32 v87, v87\n\t"
        "v_cvt_pk_bf16_f32 v138, v84, v85\n\t"
        "v_cvt_pk_bf16_f32 v139, v86, v87\n\t"
        "v_mfma_f32_32x32x16_bf16 %1, %7, v[152:155], %1\n\t"
        "ds_read_b128 %7, %11 offset:9152\n\t"
        "v_exp_f32 v88, v88\n\t"
        "v_exp_f32 v89, v89\n\t"
        "v_exp_f32 v90, v90\n\t"
        "v_exp_f32 v91, v91\n\t"
        "v_cvt_pk_bf16_f32 v140, v88, v89\n\t"
        "v_cvt_pk_bf16_f32 v141, v90, v91\n\t"
        "v_mfma_f32_32x32x16_bf16 %1, %8, v[156:159], %1\n\t"
        "s_waitcnt lgkmcnt(1)\n\t"
        "v_exp_f32 v92, v92\n\t"
        "v_exp_f32 v93, v93\n\t"
        "v_exp_f32 v94, v94\n\t"
        "v_exp_f32 v95, v95\n\t"
        "v_cvt_pk_bf16_f32 v142, v92, v93\n\t"
        "v_cvt_pk_bf16_f32 v143, v94, v95\n\t"
        "v_mfma_f32_32x32x16_bf16 v[64:79], %2, %14, %12\n\t"
        "ds_read_b128 %8, %11 offset:9824\n\t"
        "v_exp_f32 v96, v96\n\t"
        "v_exp_f32 v97, v97\n\t"
        "v_exp_f32 v98, v98\n\t"
        "v_exp_f32 v99, v99\n\t"
        "v_cvt_pk_bf16_f32 v144, v96, v97\n\t"
        "v_cvt_pk_bf16_f32 v145, v98, v99\n\t"
        "v_mfma_f32_32x32x16_bf16 v[80:95], %3, %14, %12\n\t"
        "ds_read_b64 %4, %10 offset:7296\n\t"
        "v_exp_f32 v100, v100\n\t"
        "v_exp_f32 v101, v101\n\t"
        "v_exp_f32 v102, v102\n\t"
        "v_exp_f32 v103, v103\n\t"
        "v_cvt_pk_bf16_f32 v146, v100, v101\n\t"
        "v_cvt_pk_bf16_f32 v147, v102, v103\n\t"
        "v_mfma_f32_32x32x16_bf16 %0, %5, v[128:131], %0\n\t"
        "global_load_lds_dwordx4 %18, s[40:41] offset:2048 sc1\n\t"
        "v_exp_f32 v104, v104\n\t"
        "v_exp_f32 v105, v105\n\t"
        "v_exp_f32 v106, v106\n\t"
        "v_exp_f32 v107, v107\n\t"
        "v_cvt_pk_bf16_f32 v148, v104, v105\n\t"
        "v_cvt_pk_bf16_f32 v149, v106, v107\n\t"
        "v_mfma_f32_32x32x16_bf16 %0, %6, v[132:135], %0\n\t"
        "s_waitcnt lgkmcnt(0)\n\t"
        "v_exp_f32 v108, v108\n\t"
        "v_exp_f32 v109, v109\n\t"
        "v_exp_f32 v110, v110\n\t"
        "v_exp_f32 v111, v111\n\t"
        "v_cvt_pk_bf16_f32 v150, v108, v109\n\t"
        "v_cvt_pk_bf16_f32 v151, v110, v111\n\t"
        "v_mfma_f32_32x32x4_2b_bf16 v[64:95], %4, %16, v[64:95]\n\t"
        "v_exp_f32 v112, v112\n\t"
        "v_exp_f32 v113, v113\n\t"
        "v_exp_f32 v114, v114\n\t"
        "v_exp_f32 v115, v115\n\t"
        "v_cvt_pk_bf16_f32 v152, v112, v113\n\t"
        "v_cvt_pk_bf16_f32 v153, v114, v115\n\t"
        "v_exp_f32 v116, v116\n\t"
        "v_exp_f32 v117, v117\n\t"
        "v_exp_f32 v118, v118\n\t"
        "v_exp_f32 v119, v119\n\t"
        "v_cvt_pk_bf16_f32 v154, v116, v117\n\t"
        "v_cvt_pk_bf16_f32 v155, v118, v119\n\t"
        "v_mfma_f32_32x32x16_bf16 %0, %7, v[136:139], %0\n\t"
        "global_load_lds_dwordx4 %18, s[40:41] offset:3072 sc1\n\t"
        "v_exp_f32 v120, v120\n\t"
        "v_exp_f32 v121, v121\n\t"
        "v_exp_f32 v122, v122\n\t"
        "v_exp_f32 v123, v123\n\t"
        "v_cvt_pk_bf16_f32 v156, v120, v121\n\t"
        "v_cvt_pk_bf16_f32 v157, v122, v123\n\t"
        "v_mfma_f32_32x32x16_bf16 %0, %8, v[140:143], %0\n\t"
        "s_add_u32 m0, s45, 4096\n\t"
        "v_exp_f32 v124, v124\n\t"
        "v_exp_f32 v125, v125\n\t"
        "v_exp_f32 v126, v126\n\t"
        "v_exp_f32 v127, v127\n\t"
        "v_cvt_pk_bf16_f32 v158, v124, v125\n\t"
        "v_cvt_pk_bf16_f32 v159, v126, v127\n\t"
        "v_mfma_f32_32x32x16_bf16 v[96:111], %2, %15, %13\n\t"
        "ds_read_b128 %2, %9 offset:10496\n\t"
        "v_exp_f32 v64, v64\n\t"
        "v_exp_f32 v65, v65\n\t"
        "v_exp_f32 v66, v66\n\t"
        "v_exp_f32 v67, v67\n\t"
        "v_cvt_pk_bf16_f32 v128, v64, v65\n\t"
        "v_cvt_pk_bf16_f32 v129, v66, v67\n\t"
        "v_mfma_f32_32x32x16_bf16 v[112:127], %3, %15, %13\n\t"
        "ds_read_b128 %3, %9 offset:11520\n\t"
        "v_exp_f32 v68, v68\n\t"
        "v_exp_f32 v69, v69\n\t"
        "v_exp_f32 v70, v70\n\t"
        "v_exp_f32 v71, v71\n\t"
        "v_cvt_pk_bf16_f32 v130, v68, v69\n\t"
        "v_cvt_pk_bf16_f32 v131, v70, v71\n\t"
        "v_mfma_f32_32x32x16_bf16 %1, %5, v[144:147], %1\n\t"
        "ds_read_b128 %5, %11 offset:13056\n\t"
        "v_exp_f32 v72, v72\n\t"
        "v_exp_f32 v73, v73\n\t"
        "v_exp_f32 v74, v74\n\t"
        "v_exp_f32 v75, v75\n\t"
        "v_cvt_pk_bf16_f32 v132, v72, v73\n\t"
        "v_cvt_pk_bf16_f32 v133, v74, v75\n\t"
        "v_mfma_f32_32x32x16_bf16 %1, %6, v[148:151], %1\n\t"
        "ds_read_b128 %6, %11 offset:13728\n\t"
        "v_exp_f32 v76, v76\n\t"
        "v_exp_f32 v77, v77\n\t"
        "v_exp_f32 v78, v78\n\t"
        "v_exp_f32 v79, v79\n\t"
        "v_cvt_pk_bf16_f32 v134, v76, v77\n\t"
        "v_cvt_pk_bf16_f32 v135, v78, v79\n\t"
        "v_mfma_f32_32x32x4_2b_bf16 v[96:127], %4, %17, v[96:127]\n\t"
        "v_exp_f32 v80, v80\n\t"
        "v_exp_f32 v81, v81\n\t"
        "v_exp_f32 v82, v82\n\t"
        "v_exp_f32 v83, v83\n\t"
        "v_cvt_pk_bf16_f32 v136, v80, v81\n\t"
        "v_cvt_pk_bf16_f32 v137, v82, v83\n\t"
        "v_exp_f32 v84, v84\n\t"
        "v_exp_f32 v85, v85\n\t"
        "v_exp_f32 v86, v86\n\t"
        "v_exp_f32 v87, v87\n\t"
        "v_cvt_pk_bf16_f32 v138, v84, v85\n\t"
        "v_cvt_pk_bf16_f32 v139, v86, v87\n\t"
        "v_mfma_f32_32x32x16_bf16 %1, %7, v[152:155], %1\n\t"
        "ds_read_b128 %7, %11 offset:14400\n\t"
        "v_exp_f32 v88, v88\n\t"
        "v_exp_f32 v89, v89\n\t"
        "v_exp_f32 v90, v90\n\t"
        "v_exp_f32 v91, v91\n\t"
        "v_cvt_pk_bf16_f32 v140, v88, v89\n\t"
        "v_cvt_pk_bf16_f32 v141, v90, v91\n\t"
        "v_mfma_f32_32x32x16_bf16 %1, %8, v[156:159], %1\n\t"
        "s_waitcnt lgkmcnt(1)\n\t"
        "v_exp_f32 v92, v92\n\t"
        "v_exp_f32 v93, v93\n\t"
        "v_exp_f32 v94, v94\n\t"
        "v_exp_f32 v95, v95\n\t"
        "v_cvt_pk_bf16_f32 v142, v92, v93\n\t"
        "v_cvt_pk_bf16_f32 v143, v94, v95\n\t"
        "v_mfma_f32_32x32x16_bf16 v[64:79], %2, %14, %12\n\t"
        "ds_read_b128 %8, %11 offset:15072\n\t"
        "v_exp_f32 v96, v96\n\t"
        "v_exp_f32 v97, v97\n\t"
        "v_exp_f32 v98, v98\n\t"
        "v_exp_f32 v99, v99\n\t"
        "v_cvt_pk_bf16_f32 v144, v96, v97\n\t"
        "v_cvt_pk_bf16_f32 v145, v98, v99\n\t"
        "v_mfma_f32_32x32x16_bf16 v[80:95], %3, %14, %12\n\t"
        "ds_read_b64 %4, %10 offset:12544\n\t"
        "v_exp_f32 v100, v100\n\t"
        "v_exp_f32 v101, v101\n\t"
        "v_exp_f32 v102, v102\n\t"
        "v_exp_f32 v103, v103\n\t"
        "v_cvt_pk_bf16_f32 v146, v100, v101\n\t"
        "v_cvt_pk_bf16_f32 v147, v102, v103\n\t"
        "v_mfma_f32_32x32x16_bf16 %0, %5, v[128:131], %0\n\t"
        "global_load_lds_dwordx4 %19, s[40:41] offset:0 sc1\n\t"
        "v_exp_f32 v104, v104\n\t"
        "v_exp_f32 v105, v105\n\t"
        "v_exp_f32 v106, v106\n\t"
        "v_exp_f32 v107, v107\n\t"
        "v_cvt_pk_bf16_f32 v148, v104, v105\n\t"
        "v_cvt_pk_bf16_f32 v149, v106, v107\n\t"
        "v_mfma_f32_32x32x16_bf16 %0, %6, v[132:135], %0\n\t"
        "s_waitcnt lgkmcnt(0)\n\t"
        "v_exp_f32 v108, v108\n\t"
        "v_exp_f32 v109, v109\n\t"
        "v_exp_f32 v110, v110\n\t"
        "v_exp_f32 v111, v111\n\t"
        "v_cvt_pk_bf16_f32 v150, v108, v109\n\t"
        "v_cvt_pk_bf16_f32 v151, v110, v111\n\t"
        "v_mfma_f32_32x32x4_2b_bf16 v[64:95], %4, %16, v[64:95]\n\t"
        "v_exp_f32 v112, v112\n\t"
        "v_exp_f32 v113, v113\n\t"
        "v_exp_f32 v114, v114\n\t"
        "v_exp_f32 v115, v115\n\t"
        "v_cvt_pk_bf16_f32 v152, v112, v113\n\t"
        "v_cvt_pk_bf16_f32 v153, v114, v115\n\t"
        "v_exp_f32 v116, v116\n\t"
        "v_exp_f32 v117, v117\n\t"
        "v_exp_f32 v118, v118\n\t"
        "v_exp_f32 v119, v119\n\t"
        "v_cvt_pk_bf16_f32 v154, v116, v117\n\t"
        "v_cvt_pk_bf16_f32 v155, v118, v119\n\t"
        "v_mfma_f32_32x32x16_bf16 %0, %7, v[136:139], %0\n\t"
        "s_mov_b64 exec, 0xff\n\t"
        "global_load_lds_dwordx4 %19, s[40:41] offset:1024 sc1\n\t"
        "s_mov_b64 exec, -1\n\t"
        "v_exp_f32 v120, v120\n\t"
        "v_exp_f32 v121, v121\n\t"
        "v_exp_f32 v122, v122\n\t"
        "v_exp_f32 v123, v123\n\t"
        "v_cvt_pk_bf16_f32 v156, v120, v121\n\t"
        "v_cvt_pk_bf16_f32 v157, v122, v123\n\t"
        "v_mfma_f32_32x32x16_bf16 %0, %8, v[140:143], %0\n\t"
        "s_add_u32 s40, s40, s43\n\t"
        "v_exp_f32 v124, v124\n\t"
        "v_exp_f32 v125, v125\n\t"
        "v_exp_f32 v126, v126\n\t"
        "v_exp_f32 v127, v127\n\t"
        "v_cvt_pk_bf16_f32 v158, v124, v125\n\t"
        "v_cvt_pk_bf16_f32 v159, v126, v127\n\t"
        "v_mfma_f32_32x32x16_bf16 v[96:111], %2, %15, %13\n\t"
        "ds_read_b128 %2, %9 offset:15744\n\t"
        "v_exp_f32 v64, v64\n\t"
        "v_exp_f32 v65, v65\n\t"
        "v_exp_f32 v66, v66\n\t"
        "v_exp_f32 v67, v67\n\t"
        "v_cvt_pk_bf16_f32 v128, v64, v65\n\t"
        "v_cvt_pk_bf16_f32 v129, v66, v67\n\t"
        "v_mfma_f32_32x32x16_bf16 v[112:127], %3, %15, %13\n\t"
        "ds_read_b128 %3, %9 offset:16768\n\t"
        "v_exp_f32 v68, v68\n\t"
        "v_exp_f32 v69, v69\n\t"
        "v_exp_f32 v70, v70\n\t"
        "v_exp_f32 v71, v71\n\t"
        "v_cvt_pk_bf16_f32 v130, v68, v69\n\t"
        "v_cvt_pk_bf16_f32 v131, v70, v71\n\t"
        "v_mfma_f32_32x32x16_bf16 %1, %5, v[144:147], %1\n\t"
        "ds_read_b128 %5, %11 offset:18304\n\t"
        "v_exp_f32 v72, v72\n\t"
        "v_exp_f32 v73, v73\n\t"
        "v_exp_f32 v74, v74\n\t"
        "v_exp_f32 v75, v75\n\t"
        "v_cvt_pk_bf16_f32 v132, v72, v73\n\t"
        "v_cvt_pk_bf16_f32 v133, v74, v75\n\t"
        "v_mfma_f32_32x32x16_bf16 %1, %6, v[148:151], %1\n\t"
        "ds_read_b128 %6, %11 offset:18976\n\t"
        "v_exp_f32 v76, v76\n\t"
        "v_exp_f32 v77, v77\n\t"
        "v_exp_f32 v78, v78\n\t"
        "v_exp_f32 v79, v79\n\t"
        "v_cvt_pk_bf16_f32 v134, v76, v77\n\t"
        "v_cvt_pk_bf16_f32 v135, v78, v79\n\t"
        "v_mfma_f32_32x32x4_2b_bf16 v[96:127], %4, %17, v[96:127]\n\t"
        "s_mov_b32 s42, s45\n\t"
        "v_exp_f32 v80, v80\n\t"
        "v_exp_f32 v81, v81\n\t"
        "v_exp_f32 v82, v82\n\t"
        "v_exp_f32 v83, v83\n\t"
        "v_cvt_pk_bf16_f32 v136, v80, v81\n\t"
        "v_cvt_pk_bf16_f32 v137, v82, v83\n\t"
        "v_exp_f32 v84, v84\n\t"
        "v_exp_f32 v85, v85\n\t"
        "v_exp_f32 v86, v86\n\t"
        "v_exp_f32 v87, v87\n\t"
        "v_cvt_pk_bf16_f32 v138, v84, v85\n\t"
        "v_cvt_pk_bf16_f32 v139, v86, v87\n\t"
        "v_mfma_f32_32x32x16_bf16 %1, %7, v[152:155], %1\n\t"
        "ds_read_b128 %7, %11 offset:19648\n\t"
        "v_exp_f32 v88, v88\n\t"
        "v_exp_f32 v89, v89\n\t"
        "v_exp_f32 v90, v90\n\t"
        "v_exp_f32 v91, v91\n\t"
        "v_cvt_pk_bf16_f32 v140, v88, v89\n\t"
        "v_cvt_pk_bf16_f32 v141, v90, v91\n\t"
        "v_mfma_f32_32x32x16_bf16 %1, %8, v[156:159], %1\n\t"
        "s_waitcnt lgkmcnt(1)\n\t"
        "v_exp_f32 v92, v92\n\t"
        "v_exp_f32 v93, v93\n\t"
        "v_exp_f32 v94, v94\n\t"
        "v_exp_f32 v95, v95\n\t"
        "v_cvt_pk_bf16_f32 v142, v92, v93\n\t"
        "v_cvt_pk_bf16_f32 v143, v94, v95\n\t"
        "v_mfma_f32_32x32x16_bf16 v[64:79], %2, %14, %12\n\t"
        "ds_read_b128 %8, %11 offset:20320\n\t"
        "v_exp_f32 v96, v96\n\t"
        "v_exp_f32 v97, v97\n\t"
        "v_exp_f32 v98, v98\n\t"
        "v_exp_f32 v99, v99\n\t"
        "v_cvt_pk_bf16_f32 v144, v96, v97\n\t"
        "v_cvt_pk_bf16_f32 v145, v98, v99\n\t"
        "v_mfma_f32_32x32x16_bf16 v[80:95], %3, %14, %12\n\t"
        "ds_read_b64 %4, %10 offset:17792\n\t"
        "v_exp_f32 v100, v100\n\t"
        "v_exp_f32 v101, v101\n\t"
        "v_exp_f32 v102, v102\n\t"
        "v_exp_f32 v103, v103\n\t"
        "v_cvt_pk_bf16_f32 v146, v100, v101\n\t"
        "v_cvt_pk_bf16_f32 v147, v102, v103\n\t"
        "v_mfma_f32_32x32x16_bf16 %0, %5, v[128:131], %0\n\t"
        "s_addc_u32 s41, s41, 0\n\t"
        "v_exp_f32 v104, v104\n\t"
        "v_exp_f32 v105, v105\n\t"
        "v_exp_f32 v106, v106\n\t"
        "v_exp_f32 v107, v107\n\t"
        "v_cvt_pk_bf16_f32 v148, v104, v105\n\t"
        "v_cvt_pk_bf16_f32 v149, v106, v107\n\t"
        "v_mfma_f32_32x32x16_bf16 %0, %6, v[132:135], %0\n\t"
        "s_waitcnt lgkmcnt(0)\n\t"
        "v_exp_f32 v108, v108\n\t"
        "v_exp_f32 v109, v109\n\t"
        "v_exp_f32 v110, v110\n\t"
        "v_exp_f32 v111, v111\n\t"
        "v_cvt_pk_bf16_f32 v150, v108, v109\n\t"
        "v_cvt_pk_bf16_f32 v151, v110, v111\n\t"
        "v_mfma_f32_32x32x4_2b_bf16 v[64:95], %4, %16, v[64:95]\n\t"
        "s_mov_b32 s45, s47\n\t"
        "s_sub_u32 s49, s48, s47\n\t"
        "s_mov_b32 s47, s48\n\t"
        "s_mov_b32 s48, s42\n\t"
        "v_exp_f32 v112, v112\n\t"
        "v_exp_f32 v113, v113\n\t"
        "v_exp_f32 v114, v114\n\t"
        "v_exp_f32 v115, v115\n\t"
        "v_cvt_pk_bf16_f32 v152, v112, v113\n\t"
        "v_cvt_pk_bf16_f32 v153, v114, v115\n\t"
        "v_exp_f32 v116, v116\n\t"
        "v_exp_f32 v117, v117\n\t"
        "v_exp_f32 v118, v118\n\t"
        "v_exp_f32 v119, v119\n\t"
        "v_cvt_pk_bf16_f32 v154, v116, v117\n\t"
        "v_cvt_pk_bf16_f32 v155, v118, v119\n\t"
        "v_mfma_f32_32x32x16_bf16 %0, %7, v[136:139], %0\n\t"
        "s_waitcnt vmcnt(6)\n\t"
        "v_exp_f32 v120, v120\n\t"
        "v_exp_f32 v121, v121\n\t"
        "v_exp_f32 v122, v122\n\t"
        "v_exp_f32 v123, v123\n\t"
        "v_cvt_pk_bf16_f32 v156, v120, v121\n\t"
        "v_cvt_pk_bf16_f32 v157, v122, v123\n\t"
        "v_mfma_f32_32x32x16_bf16 %0, %8, v[140:143], %0\n\t"
        "s_barrier\n\t"
        "v_exp_f32 v124, v124\n\t"
        "v_exp_f32 v125, v125\n\t"
        "v_exp_f32 v126, v126\n\t"
        "v_exp_f32 v127, v127\n\t"
        "v_cvt_pk_bf16_f32 v158, v124, v125\n\t"
        "v_cvt_pk_bf16_f32 v159, v126, v127\n\t"
        "v_add_u32 %9, s49, %9\n\t"
        "v_add_u32 %10, s49, %10\n\t"
        "v_add_u32 %11, s49, %11\n\t"
        "s_cmp_lg_u32 s44, 0\n\t"
        "s_cbranch_scc1 0b\n\t"
        "v_mfma_f32_32x32x16_bf16 %1, %5, v[144:147], %1\n\t"
        "v_mfma_f32_32x32x16_bf16 %1, %6, v[148:151], %1\n\t"
        "v_mfma_f32_32x32x16_bf16 %1, %7, v[152:155], %1\n\t"
        "v_mfma_f32_32x32x16_bf16 %1, %8, v[156:159], %1\n\t"
        "s_mov_b32 m0, s46\n\t"
        "s_setprio 3\n\t"
        "s_nop 15\n\t"
        : "+v"(o[0]), "+v"(o[1]), "+v"(k0x), "+v"(k0y), "+v"(k1), "+v"(v0), "+v"(v1), "+v"(v2), "+v"(v3), "+v"(aK), "+v"(aK1), "+v"(aV)
        : "v"(ca), "v"(cb), "v"(q0[0]), "v"(q0[1]), "v"(q1[0]), "v"(q1[1]), "v"(voff), "v"(voff + 4096),
          "s"(kvbase + ST + 2 * A_CHUNK), "s"(lds0 + wid * ST)
        :
          "v64", "v65", "v66", "v67", "v68", "v69", "v70", "v71", "v72", "v73", "v74", "v75", "v76", "v77", "v78", "v79",
          "v80", "v81", "v82", "v83", "v84", "v85", "v86", "v87", "v88", "v89", "v90", "v91", "v92", "v93", "v94", "v95",
          "v96", "v97", "v98", "v99", "v100", "v101", "v102", "v103", "v104", "v105", "v106", "v107", "v108", "v109", "v110", "v111",
          "v112", "v113", "v114", "v115", "v116", "v117", "v118", "v119", "v120", "v121", "v122", "v123", "v124", "v125", "v126", "v127",
          "v128", "v129", "v130", "v131", "v132", "v133", "v134", "v135", "v136", "v137", "v138", "v139", "v140", "v141", "v142", "v143",
          "v144", "v145", "v146", "v147", "v148", "v149", "v150", "v151", "v152", "v153", "v154", "v155", "v156", "v157", "v158", "v159",
          "s40", "s41", "s42", "s43", "s44", "s45", "s46", "s47", "s48", "s49", "scc", "memory");
        bool bad = false;
#pragma unroll
        for (int i = 0; i < 2; ++i) {
            float s = 0.f;
#pragma unroll
            for (int r = 0; r < 12; ++r) s += fabsf(o[i][r]);
            bad |= !(s < 1e30f);
        }
        if (__builtin_expect(__any(bad), 0)) attn_slow(kvbase, q0, q1, o);
    }
    {
        unsigned rem[2][2];
#pragma unroll
        for (int i = 0; i < 2; ++i) {
            auto rr = __builtin_amdgcn_permlane32_swap(__float_as_uint(o[i][8]), __float_as_uint(o[i][8]), false, false);
            const float inv = __builtin_amdgcn_rcpf(__uint_as_float(rr[1]));
#define PK(a) min(cvtpk(o[i][a] * inv, o[i][(a) + 1] * inv), 0xfffffffeu)
            st16(rs, om + i * 81920, u32x4{PK(0), PK(2), PK(4), PK(6)});
            rem[i][0] = PK(8); rem[i][1] = PK(10);
#undef PK
        }
        auto r0 = __builtin_amdgcn_permlane32_swap(rem[0][0], rem[1][0], false, false);
        auto r1 = __builtin_amdgcn_permlane32_swap(rem[0][1], rem[1][1], false, false);
        st8(rs, orem, u32x2{r0[0], r1[0]});
    }
    if (lane == 0) set_flag(ws, (isX ? F_OB : F_OA) + ((tb0 >> 1) * 32 + head) * 8, tag);
    if (!isX) {
        if (b == 256) {
            __builtin_amdgcn_s_setprio(0);
            for (unsigned spins = 0; spins < kSpinCap; ++spins) {
                bool ok = true;
#pragma unroll
                for (int i = 0; i < 8; ++i) ok &= __hip_atomic_load((gu64*)(ws + F_OB + (i * 256 + tid) * 8), RLX_AGENT) == tag;
                if (__all(ok)) break;
                __builtin_amdgcn_s_sleep(16);
            }
            LDS_BAR();
            if (tid == 0) set_flag(ws, F_EPOCH, tag);
        }
        return;
    }
    __builtin_amdgcn_s_setprio(0);

    {
        const int kq = wid;
        const unsigned l16 = lane * 16;
        constexpr int FO = 20480, SO = -20480;
        const char* wa = ws + WOT_OFF + (size_t)(hg * 5) * 40960 + kq * 4096 + FO;
        const char* wa2 = ws + WOT_OFF + (size_t)(hg * 5) * 40960 + (16 + kq) * 1024 + FO;
        const char* oa = ws + OIMG_OFF + (size_t)(rb64 * 4) * 40960 + kq * 4096 + FO;
        const char* oa2 = ws + OIMG_OFF + (size_t)(rb64 * 4) * 40960 + (16 + kq) * 1024 + FO;
        const unsigned f2nd = F_OA + (rb64 * 32 + kq * 8 + (lane & 7)) * 8;
        int so[4];
#pragma unroll
        for (int i = 0; i < 4; ++i) so[i] = ((kq + i) & 3) * 40960;
        bf16x8 wf[5][5];
        u32x4 of[5][4];
        f32x4 acc[4][5];
        wait_flags(ws, F_AUX + xcd * F_REP + lane * 8, tag);
        const float g = __uint_as_float(__hip_atomic_load((gu32*)(ws + G_OFF + (rb64 * 64 + kq * 16 + (lane & 15)) * 4), RLX_AGENT));
        out_load_w(wa, wa2, l16, wf);
        if (wid == 0) wait_flags(ws, F_OB + (rb64 * 32 + r32) * 8, tag);
        WAIT_BAR(0);
#define LOAD_O(BR) _Pragma("unroll") for (int ks = 0; ks < 5; ++ks) _Pragma("unroll") for (int i = 0; i < 4; ++i) \
            asm volatile("global_load_dwordx4 %0, %1, %2 offset:%c3 sc1" : "=v"(of[ks][i]) : "v"(l16), "s"((ks < 4 ? oa : oa2) + (BR) * SO + so[i]), "i"(KSOFF(ks)) : "memory")
#define LOAD_W2(ks) _Pragma("unroll") for (int jj = 0; jj < 5; ++jj) \
            asm volatile("global_load_dwordx4 %0, %1, %2 offset:%c3 sc1" : "=v"(wf[ks][jj]) : "v"(l16), "s"(((ks) < 4 ? wa : wa2) + SO + jj * 40960), "i"(KSOFF(ks)) : "memory")
#define OSTEP(N, ks) asm volatile("s_waitcnt vmcnt(" #N ")" : "+v"(of[ks][0]), "+v"(of[ks][1]), "+v"(of[ks][2]), "+v"(of[ks][3]), \
            "+v"(wf[ks][0]), "+v"(wf[ks][1]), "+v"(wf[ks][2]), "+v"(wf[ks][3]), "+v"(wf[ks][4])); \
        _Pragma("unroll") for (int i = 0; i < 4; ++i) { \
            _Pragma("unroll") for (int jj = 0; jj < 5; ++jj) acc[i][jj] = MFMA16(wf[ks][jj], __builtin_bit_cast(bf16x8, of[ks][i]), (ks) == 0 ? f32x4{} : acc[i][jj]); \
            mx = max(max(mx, max(of[ks][i][0], of[ks][i][1])), max(of[ks][i][2], of[ks][i][3])); } \
        SBAR()
#define PUT_PARTIALS() _Pragma("unroll") for (int i = 1; i < 4; ++i) { char* pp_ = lds + (((kq + i) & 3) * 3 + i - 1) * O_STRIP + lane * 16; \
            _Pragma("unroll") for (int jj = 0; jj < 5; ++jj) *(f32x4*)(pp_ + jj * 1024) = acc[i][jj]; }
#define SUM_STRIP(jj) (acc[0][jj] + *(const f32x4*)(rp + (jj) * 1024) + *(const f32x4*)(rp + O_STRIP + (jj) * 1024) + *(const f32x4*)(rp + 2 * O_STRIP + (jj) * 1024))
        const char* rp = lds + (kq * 3) * O_STRIP + lane * 16;
        char* keep = lds + 12 * O_STRIP + kq * O_STRIP + lane * 16;
        bool early;
        for (unsigned spins = 0; spins < kSpinCap; ++spins) {
            if (spins) { asm volatile("s_waitcnt vmcnt(0)" ::: "memory"); out_load_w(wa, wa2, l16, wf); }
            unsigned long long peekv;
            asm volatile("global_load_dwordx2 %0, %1, %2 sc1" : "=v"(peekv) : "v"(f2nd), "s"(ws) : "memory");
            LOAD_O(0);
            asm volatile("s_waitcnt vmcnt(20)" : "+v"(peekv));
            early = __all(peekv == tag);
            unsigned mx = 0;
            OSTEP(16, 0); OSTEP(12, 1); LOAD_W2(0); OSTEP(13, 2); LOAD_W2(1); OSTEP(14, 3); LOAD_W2(2); OSTEP(15, 4); LOAD_W2(3); LOAD_W2(4);
            if (!__any(mx == ~0u)) break;
        }
        PUT_PARTIALS();
        if (!early) {
            LDS_BAR();
#pragma unroll
            for (int jj = 0; jj < 5; ++jj) *(f32x4*)(keep + jj * 1024) = SUM_STRIP(jj);
            __builtin_amdgcn_s_setprio(0);
            wait_flags(ws, f2nd, tag);
        } else {
#pragma unroll
            for (int jj = 0; jj < 5; ++jj) *(f32x4*)(keep + jj * 1024) = acc[0][jj];
        }
        for (unsigned spins = 0; spins < kSpinCap; ++spins) {
            if (spins) out_load_w(wa + SO, wa2 + SO, l16, wf);
            LOAD_O(1);
            if (early && !spins) {
                LDS_BAR();
#pragma unroll
                for (int jj = 0; jj < 5; ++jj)
                    *(f32x4*)(keep + jj * 1024) = *(const f32x4*)(keep + jj * 1024) + *(const f32x4*)(rp + jj * 1024) + *(const f32x4*)(rp + O_STRIP + jj * 1024) + *(const f32x4*)(rp + 2 * O_STRIP + jj * 1024);
            }
            unsigned mx = 0;
            OSTEP(16, 0); OSTEP(12, 1); OSTEP(8, 2); OSTEP(4, 3); OSTEP(0, 4);
            if (!__any(mx == ~0u)) break;
        }
        LDS_BAR();
        PUT_PARTIALS();
        LDS_BAR();
        const float omg = rbf(1.f - g);
        unsigned short* op2 = out + (size_t)(rb64 * 64 + kq * 16 + (lane & 15)) * DM + hg * 80 + (lane >> 4) * 4;
#pragma unroll
        for (int jj = 0; jj < 5; ++jj) {
            const f32x4 bb = *(const f32x4*)(keep + jj * 1024);
            const f32x4 aa = SUM_STRIP(jj);
            float r[4];
#pragma unroll
            for (int e = 0; e < 4; ++e) r[e] = rbf(g * rbf(aa[e])) + rbf(omg * rbf(bb[e]));
            *(u32x2*)(op2 + jj * 16) = u32x2{cvtpk(r[0], r[1]), cvtpk(r[2], r[3])};
        }
    }
    asm volatile(".Lfk_end:");
}

extern "C" void kernel_launch(void* const* d_in, const int* in_sizes, int n_in, void* d_out, int out_size, void* d_ws,
                              size_t ws_size, hipStream_t stream) {
    (void)hipFuncSetAttribute((const void*)fused_kernel, hipFuncAttributeMaxDynamicSharedMemorySize, LDS_BYTES);
    fused_kernel<<<512, 256, LDS_BYTES, stream>>>(
        (const unsigned short*)d_in[0], (const unsigned short*)d_in[1], (const unsigned short*)d_in[4], (const unsigned short*)d_in[5],
        (const unsigned short*)d_in[6], (const unsigned short*)d_in[2], (const unsigned short*)d_in[7], (const unsigned short*)d_in[8],
        (char*)d_ws, (unsigned short*)d_out);
}
```
